```python
import torch

def _softmax(x, axis=-1):
    return torch.softmax(x, dim=axis)
def _gelu(x):
    return torch.nn.functional.gelu(x, approximate="tanh")
def _silu(x):
    return torch.nn.functional.silu(x)
def _sigmoid(x):
    return torch.sigmoid(x)
def _layernorm(x, axis=-1, eps=1e-5):
    m = x.mean(dim=axis, keepdim=True)
    v = x.var(dim=axis, keepdim=True, unbiased=False)
    return (x - m) / torch.sqrt(v + eps)
def _scatter_add(tgt, idx, src, axis=0):
    return tgt.clone().index_add_(axis, idx, src)
def _segment_sum(src, seg, n):
    out = torch.zeros((n,) + src.shape[1:], dtype=src.dtype)
    return out.index_add_(0, seg, src)
def _pad(x, pw):
    flat = tuple(v for pair in reversed(pw) for v in pair)
    return torch.nn.functional.pad(x, flat)

kernel_name = 'parallel_sum__sliding_local_dilated__glu_gated_swiglu__prompt'

def setup_inputs():

    _w = (lambda a: (lambda t: t.to(torch.bfloat16) if t.is_floating_point() else t)(torch.as_tensor(a)))
    def _A_setup():
        B, S, D = 1, 2048, 640
        return {
            "x": _w((lambda *s: torch.randn(*s, dtype=torch.bfloat16) * 0.05)(B, S, D)),
            "wqkv": _w((lambda *s: torch.randn(*s, dtype=torch.bfloat16) * 0.05)(D, 3 * D)),
            "wo": _w((lambda *s: torch.randn(*s, dtype=torch.bfloat16) * 0.05)(D, D)),
        }
    _A_in = _A_setup()

    def _B_setup():
        B, S, D, F = 1, 2048, 640, 1536
        return {
            "x": _w((lambda *s: torch.randn(*s, dtype=torch.bfloat16) * 0.05)(B, S, D)),
            "w1": _w((lambda *s: torch.randn(*s, dtype=torch.bfloat16) * 0.8)(D, F) * (2.0 / D) ** 0.5),
            "w3": _w((lambda *s: torch.randn(*s, dtype=torch.bfloat16) * 0.8)(D, F) * (2.0 / D) ** 0.5),
            "w2": _w((lambda *s: torch.randn(*s, dtype=torch.bfloat16) * 0.8)(F, D) * (2.0 / F) ** 0.5),
        }
    _B_in = _B_setup()

    return {
        "A_x": _A_in["x"],
        "A_wqkv": _A_in["wqkv"],
        "A_wo": _A_in["wo"],
        "B_x": _B_in["x"],
        "B_w1": _B_in["w1"],
        "B_w3": _B_in["w3"],
        "B_w2": _B_in["w2"],
    }

def reference(A_x, A_wqkv, A_wo, B_x, B_w1, B_w3, B_w2):
    def _A_ref(x, wqkv, wo):
        B, S, D = x.shape
        H = 32; Dh = D // H
        W, dil = 8, 2
        qkv = (lambda x, *d: x.permute(*d))((x @ wqkv).reshape(B, S, 3, H, Dh), 2, 0, 3, 1, 4)
        q, k, v = qkv[0], qkv[1], qkv[2]
        scale = Dh ** -0.5
        pos = torch.arange(S)
        off = torch.arange(W) * dil
        idx = pos[:, None] - off[None, :]
        mask = idx >= 0
        idx_c = torch.where(mask, idx, 0).reshape(-1)
        kg = k[:, :, idx_c, :].reshape(B, H, S, W, Dh)
        vg = v[:, :, idx_c, :].reshape(B, H, S, W, Dh)
        logits = torch.einsum("bhsd,bhswd->bhsw", q, kg) * scale
        logits = torch.where(mask[None, None, :, :], logits, -1e9)
        attn = _softmax(logits, axis=-1)
        o = torch.einsum("bhsw,bhswd->bhsd", attn, vg)
        return (lambda x, *d: x.permute(*d))(o, 0, 2, 1, 3).reshape(B, S, D) @ wo

    def _B_ref(x, w1, w3, w2):
        gate = torch.einsum("bsd,df->bsf", x, w1)
        up = torch.einsum("bsd,df->bsf", x, w3)
        h = _silu(gate) * up
        out = torch.einsum("bsf,fd->bsd", h, w2)
        return out

    x = A_x
    xn = _layernorm(x, axis=-1)
    return x + _A_ref(xn, A_wqkv, A_wo) + _B_ref(xn, B_w1, B_w3, B_w2)
```

```cpp
#include <hip/hip_runtime.h>
#include <hip/hip_bf16.h>
#include <stdint.h>

typedef __attribute__((ext_vector_type(8))) short bf16x8;
typedef __attribute__((ext_vector_type(4))) short s16x4;
typedef __attribute__((ext_vector_type(2))) __bf16 bf16x2v;
typedef __attribute__((ext_vector_type(4))) float f32x4;
typedef __attribute__((ext_vector_type(16))) float f32x16;
typedef __attribute__((ext_vector_type(4))) unsigned u32x4;
typedef __attribute__((ext_vector_type(2))) unsigned u32x2;
typedef unsigned short u16;
#define LDS_AS __attribute__((address_space(3)))

namespace {

constexpr int S_ = 2048, D_ = 640, F_ = 1536;
constexpr int KS2 = 68;
constexpr size_t ACT_BYTES = (size_t)128 * KS2 * 1024;
constexpr size_t WF_OFF = ACT_BYTES;

constexpr int XSLOT = 24576, NXS = 3;
constexpr int WSLOT = 16384, NWS = 5;
constexpr int WRING = XSLOT * NXS;
constexpr int K1_LDS = WRING + WSLOT * NWS;
constexpr int P_STAT = 0;
constexpr int P_CS = 12288;
constexpr int P_ROW = 13312;
constexpr int P_QKV = 16384;
constexpr int QKV_STRIDE = 488;

#define WAIT_V(n) asm volatile("s_waitcnt vmcnt(%0)" ::"n"(n) : "memory")
#define WAIT_L0() asm volatile("s_waitcnt lgkmcnt(0)" ::: "memory")
#define RAW_BARRIER() do { WAIT_L0(); __builtin_amdgcn_s_barrier(); } while (0)

__device__ __forceinline__ float bfr(float f) { return (float)(__bf16)f; }
__device__ __forceinline__ unsigned pack2(float a, float b) {
  bf16x2v v; v[0] = (__bf16)a; v[1] = (__bf16)b; return __builtin_bit_cast(unsigned, v);
}
__device__ __forceinline__ float bflo(unsigned u) { return __builtin_bit_cast(float, u << 16); }
__device__ __forceinline__ float bfhi(unsigned u) { return __builtin_bit_cast(float, u & 0xffff0000u); }
__device__ __forceinline__ float dot2(unsigned a, unsigned b, float c) {
  return __builtin_amdgcn_fdot2_f32_bf16(__builtin_bit_cast(bf16x2v, a), __builtin_bit_cast(bf16x2v, b), c, false);
}
__device__ __forceinline__ void dma16(const void* g, char* lds_uniform) {
  __builtin_amdgcn_global_load_lds((const unsigned*)g, (unsigned*)lds_uniform, 16, 0, 0);
}
__device__ __forceinline__ s16x4 tr_read(const char* p) {
  return __builtin_amdgcn_ds_read_tr16_b64_v4i16((LDS_AS s16x4*)p);
}

template <bool QKV>
__device__ __forceinline__ void k1_body(char* smem, const u16* __restrict__ x, const u16* __restrict__ wa,
                                        const u16* __restrict__ wb, char* __restrict__ act, int rb, int cb) {
  constexpr int MT = QKV ? 6 : 5;
  constexpr int BMR = MT * 32;
  const int tid = threadIdx.x, lane = tid & 63;
  const int wave = __builtin_amdgcn_readfirstlane(tid >> 6);
  const int h = lane >> 5, r32 = lane & 31;
  const int row0 = QKV ? rb * 176 - 16 : rb * 160;

  const char* xsrc[3];
#pragma unroll
  for (int j = 0; j < 3; ++j) {
    int lr = 8 * (wave + 8 * j) + (lane >> 3);
    int c = (lane & 7) ^ ((lr >> 1) & 7);
    int gr = row0 + lr; gr = gr < 0 ? 0 : (gr > S_ - 1 ? S_ - 1 : gr);
    xsrc[j] = (const char*)x + (size_t)gr * (D_ * 2) + c * 16;
  }
  const bool x3 = QKV || (wave < 4);
  const char* wsrc[2];
#pragma unroll
  for (int j = 0; j < 2; ++j) {
    int k = 4 * wave + 2 * j + (lane >> 5);
    int c = (lane & 31) ^ ((k & 3) << 2);
    if (QKV) {
      int cc = c > 29 ? 29 : c;
      int seg = cc / 10, cq = cc - seg * 10;
      wsrc[j] = (const char*)wa + ((size_t)k * 1920 + seg * 640 + cb * 80 + cq * 8) * 2;
    } else {
      int wv = c >> 2, part = (c >> 1) & 1, hf = c & 1;
      wsrc[j] = (const char*)(part ? wb : wa) + ((size_t)k * F_ + cb * 128 + wv * 16 + hf * 8) * 2;
    }
  }
  constexpr int WADV = (QKV ? 1920 : F_) * 2 * 32;

  auto issueW = [&](int slot) {
    char* dst = smem + WRING + slot * WSLOT + wave * 2048;
    dma16(wsrc[0], dst); dma16(wsrc[1], dst + 1024);
    wsrc[0] += WADV; wsrc[1] += WADV;
  };
  auto issueX = [&](int slot) {
    char* dst = smem + slot * XSLOT + wave * 1024;
    dma16(xsrc[0], dst); dma16(xsrc[1], dst + 8192);
    if (x3) dma16(xsrc[2], dst + 16384);
    xsrc[0] += 128; xsrc[1] += 128; xsrc[2] += 128;
  };

  int xoff[4];
#pragma unroll
  for (int q = 0; q < 4; ++q) xoff[q] = r32 * 128 + (((2 * q + h) ^ ((r32 >> 1) & 7)) * 16);
  int woff;
  {
    int blk = (lane >> 4) & 1, q4 = (lane & 15) >> 2, p = lane & 3;
    woff = WRING + (8 * h + q4) * 512 + (((4 * wave + 2 * blk + (p >> 1)) ^ (q4 << 2)) * 16) + (p & 1) * 8;
  }

  f32x16 acc[MT];
#pragma unroll
  for (int m = 0; m < MT; ++m)
#pragma unroll
    for (int i = 0; i < 16; ++i) acc[m][i] = 0.f;
  float sx[MT], sxx[MT], cs = 0.f;
#pragma unroll
  for (int m = 0; m < MT; ++m) { sx[m] = 0.f; sxx[m] = 0.f; }

  bf16x8 xf0[MT], xf1[MT], wf0, wf1;
  auto ldx = [&](bf16x8(&xf)[MT], int xbase, int q) {
#pragma unroll
    for (int m = 0; m < MT; ++m) xf[m] = *(const bf16x8*)(smem + xbase + xoff[q] + m * 4096);
  };
  auto ldw = [&](bf16x8& wf, int wbase, int k16) {
    s16x4 lo = tr_read(smem + wbase + woff + k16 * 8192);
    s16x4 hi = tr_read(smem + wbase + woff + k16 * 8192 + 2048);
    wf = __builtin_shufflevector(lo, hi, 0, 1, 2, 3, 4, 5, 6, 7);
  };
  const unsigned ONES = 0x3f803f80u;
  auto mma = [&](bf16x8(&xf)[MT], bf16x8& wf, int u) {
#pragma unroll
    for (int m = 0; m < MT; ++m) acc[m] = __builtin_amdgcn_mfma_f32_32x32x16_bf16(wf, xf[m], acc[m], 0, 0, 0);
    u32x4 wv = __builtin_bit_cast(u32x4, wf);
    cs = dot2(wv[0], ONES, cs); cs = dot2(wv[1], ONES, cs); cs = dot2(wv[2], ONES, cs); cs = dot2(wv[3], ONES, cs);
    if ((u & 7) == wave) {
#pragma unroll
      for (int m = 0; m < MT; ++m) {
        u32x4 v = __builtin_bit_cast(u32x4, xf[m]);
#pragma unroll
        for (int i = 0; i < 4; ++i) { sx[m] = dot2(v[i], ONES, sx[m]); sxx[m] = dot2(v[i], v[i], sxx[m]); }
      }
    }
  };

  issueW(0); issueX(0); issueW(1); issueW(2); issueX(1); issueW(3);
  if (x3) WAIT_V(9); else WAIT_V(8);
  RAW_BARRIER();
  ldx(xf0, 0, 0); ldw(wf0, 0, 0);

  int ws0 = 0;
  int xs0 = 0;
  for (int s = 0; s < 20; s += 2) {
    {
      if (s <= 16) { if (x3) WAIT_V(7); else WAIT_V(6); } else { WAIT_V(0); }
      RAW_BARRIER();
      int ws4 = ws0 + 4; if (ws4 >= NWS) ws4 -= NWS;
      if (s < 16) issueW(ws4);
      int xs2 = xs0 + 2; if (xs2 >= NXS) xs2 -= NXS;
      if (s <= 14) issueX(xs2);
      int ws1 = ws0 + 1; if (ws1 >= NWS) ws1 -= NWS;
      ldx(xf1, xs0 * XSLOT, 1); ldw(wf1, ws0 * WSLOT, 1);
      mma(xf0, wf0, 2 * s);
      ldx(xf0, xs0 * XSLOT, 2); ldw(wf0, ws1 * WSLOT, 0);
      mma(xf1, wf1, 2 * s + 1);
      ws0 = ws1;
    }
    {
      const int so = s + 1;
      if (so < 19) {
        if (so <= 15) { if (x3) WAIT_V(7); else WAIT_V(6); } else { WAIT_V(2); }
        RAW_BARRIER();
      }
      int ws4 = ws0 + 4; if (ws4 >= NWS) ws4 -= NWS;
      if (so < 16) issueW(ws4);
      int ws1 = ws0 + 1; if (ws1 >= NWS) ws1 -= NWS;
      int xs1 = xs0 + 1; if (xs1 >= NXS) xs1 -= NXS;
      ldx(xf1, xs0 * XSLOT, 3); ldw(wf1, ws0 * WSLOT, 1);
      mma(xf0, wf0, 2 * so);
      if (so < 19) { ldx(xf0, xs1 * XSLOT, 0); ldw(wf0, ws1 * WSLOT, 0); }
      mma(xf1, wf1, 2 * so + 1);
      ws0 = ws1; xs0 = xs1;
    }
  }

  RAW_BARRIER();
  {
    float2* pst = (float2*)(smem + P_STAT) + wave * 192;
#pragma unroll
    for (int m = 0; m < MT; ++m) {
      float a = sx[m] + __shfl_xor(sx[m], 32), b = sxx[m] + __shfl_xor(sxx[m], 32);
      if (h == 0) pst[m * 32 + r32] = make_float2(a, b);
    }
    float c = cs + __shfl_xor(cs, 32);
    if (h == 0) ((float*)(smem + P_CS))[wave * 32 + r32] = c;
  }
  __syncthreads();
  if (tid < BMR) {
    float a = 0.f, b = 0.f;
#pragma unroll
    for (int w = 0; w < 8; ++w) { float2 v = ((const float2*)(smem + P_STAT))[w * 192 + tid]; a += v.x; b += v.y; }
    float mean = a * (1.f / D_);
    float var = b * (1.f / D_) - mean * mean; var = var < 0.f ? 0.f : var;
    float mb = bfr(mean);
    float den = bfr(sqrtf(bfr(bfr(var) + 1e-5f)));
    ((float2*)(smem + P_ROW))[tid] = make_float2(mb, 1.f / den);
  }
  __syncthreads();
  float csv[16];
#pragma unroll
  for (int q = 0; q < 4; ++q) {
    f32x4 v = *(const f32x4*)(smem + P_CS + wave * 128 + (8 * q + 4 * h) * 4);
    csv[4 * q] = v[0]; csv[4 * q + 1] = v[1]; csv[4 * q + 2] = v[2]; csv[4 * q + 3] = v[3];
  }

  if (!QKV) {
    const int ks = 20 + cb * 4 + (wave >> 1);
    const int slot = (2 * (wave & 1) + h) * 16 + (r32 & 15);
#pragma unroll
    for (int m = 0; m < MT; ++m) {
      float2 rs = ((const float2*)(smem + P_ROW))[m * 32 + r32];
      float hv[8];
#pragma unroll
      for (int i = 0; i < 8; ++i) {
        float g = bfr((acc[m][i] - rs.x * csv[i]) * rs.y);
        float u = bfr((acc[m][8 + i] - rs.x * csv[8 + i]) * rs.y);
        float sg = bfr(g / (1.f + __expf(-g)));
        hv[i] = sg * u;
      }
      int grow = row0 + m * 32 + r32;
      if (grow < S_) {
        u32x4 o; o[0] = pack2(hv[0], hv[1]); o[1] = pack2(hv[2], hv[3]); o[2] = pack2(hv[4], hv[5]); o[3] = pack2(hv[6], hv[7]);
        *(u32x4*)(act + ((size_t)(grow >> 4) * KS2 + ks) * 1024 + slot * 16) = o;
      }
    }
  } else {
#pragma unroll
    for (int m = 0; m < MT; ++m) {
      float2 rs = ((const float2*)(smem + P_ROW))[m * 32 + r32];
#pragma unroll
      for (int q = 0; q < 4; ++q) {
        int col = 32 * wave + 8 * q + 4 * h;
        float v0 = (acc[m][4 * q] - rs.x * csv[4 * q]) * rs.y, v1 = (acc[m][4 * q + 1] - rs.x * csv[4 * q + 1]) * rs.y;
        float v2 = (acc[m][4 * q + 2] - rs.x * csv[4 * q + 2]) * rs.y, v3 = (acc[m][4 * q + 3] - rs.x * csv[4 * q + 3]) * rs.y;
        u32x2 o; o[0] = pack2(v0, v1); o[1] = pack2(v2, v3);
        if (col < 240) *(u32x2*)(smem + P_QKV + (m * 32 + r32) * QKV_STRIDE + col * 2) = o;
      }
    }
    __syncthreads();
    const float scale = 0.22360679774997896f;
    for (int pi = tid; pi < 704; pi += 512) {
      int hl = pi / 176, rl = pi - hl * 176;
      int s = rb * 176 + rl;
      if (s >= S_) continue;
      const char* qrow = smem + P_QKV + (16 + rl) * QKV_STRIDE + hl * 40;
      unsigned qv[10];
#pragma unroll
      for (int i = 0; i < 5; ++i) { u32x2 t = *(const u32x2*)(qrow + i * 8); qv[2 * i] = t[0]; qv[2 * i + 1] = t[1]; }
      float lg[8];
#pragma unroll
      for (int w = 0; w < 8; ++w) {
        const char* krow = qrow - 2 * w * QKV_STRIDE + 160;
        float d = 0.f;
#pragma unroll
        for (int i = 0; i < 5; ++i) { u32x2 t = *(const u32x2*)(krow + i * 8); d = dot2(qv[2 * i], t[0], d); d = dot2(qv[2 * i + 1], t[1], d); }
        lg[w] = (s - 2 * w >= 0) ? bfr(bfr(d) * scale) : -1e9f;
      }
      float mx = lg[0];
#pragma unroll
      for (int w = 1; w < 8; ++w) mx = fmaxf(mx, lg[w]);
      float sum = 0.f;
#pragma unroll
      for (int w = 0; w < 8; ++w) { lg[w] = __expf(lg[w] - mx); sum += lg[w]; }
      float inv = 1.f / sum;
      float o[20];
#pragma unroll
      for (int d = 0; d < 20; ++d) o[d] = 0.f;
#pragma unroll
      for (int w = 0; w < 8; ++w) {
        float p = bfr(lg[w] * inv);
        const char* vrow = qrow - 2 * w * QKV_STRIDE + 320;
#pragma unroll
        for (int i = 0; i < 5; ++i) {
          u32x2 t = *(const u32x2*)(vrow + i * 8);
          o[4 * i] += p * bflo(t[0]); o[4 * i + 1] += p * bfhi(t[0]);
          o[4 * i + 2] += p * bflo(t[1]); o[4 * i + 3] += p * bfhi(t[1]);
        }
      }
      char* obase = act + (size_t)(s >> 4) * KS2 * 1024 + (s & 15) * 16;
#pragma unroll
      for (int i = 0; i < 5; ++i) {
        int col = cb * 80 + hl * 20 + 4 * i;
        int ks = col >> 5, kk = col & 31;
        int g = 2 * (kk >> 4) + ((kk >> 2) & 1), jh = (kk >> 3) & 1;
        u32x2 t; t[0] = pack2(o[4 * i], o[4 * i + 1]); t[1] = pack2(o[4 * i + 2], o[4 * i + 3]);
        *(u32x2*)(obase + ks * 1024 + g * 256 + jh * 8) = t;
      }
    }
  }
}

__global__ __launch_bounds__(512, 2) void k1_kernel(const u16* __restrict__ x, const u16* __restrict__ wqkv,
                                                    const u16* __restrict__ wo, const u16* __restrict__ w1,
                                                    const u16* __restrict__ w3, const u16* __restrict__ w2,
                                                    char* __restrict__ ws) {
  __shared__ __attribute__((aligned(16))) char smem[K1_LDS];
  const int b = blockIdx.x, xcd = b & 7, idx = b >> 3;
  if (idx < 12) {
    k1_body<true>(smem, x, wqkv, wqkv, ws, idx, xcd);
  } else {
    const int t = xcd * 20 + (idx - 12);
    if (t >= 156) return;
    k1_body<false>(smem, x, w1, w3, ws, t % 13, t / 13);
    char* wf = ws + WF_OFF;
    for (int sl = t * 512 + threadIdx.x; sl < KS2 * 40 * 64; sl += 156 * 512) {
      int frag = sl >> 6, l = sl & 63;
      int ks = frag / 40, nt = frag - ks * 40;
      int g = l >> 4, c = l & 15;
      int k0 = 32 * ks + 16 * (g >> 1) + 4 * (g & 1);
      const u16* src = (ks < 20 ? wo + (size_t)k0 * D_ : w2 + (size_t)(k0 - D_) * D_) + nt * 16 + c;
      unsigned e[8];
#pragma unroll
      for (int j = 0; j < 8; ++j) e[j] = src[(size_t)(8 * (j >> 2) + (j & 3)) * D_];
      u32x4 o; o[0] = e[0] | (e[1] << 16); o[1] = e[2] | (e[3] << 16); o[2] = e[4] | (e[5] << 16); o[3] = e[6] | (e[7] << 16);
      *(u32x4*)(wf + (size_t)sl * 16) = o;
    }
  }
}

__global__ __launch_bounds__(256, 1) void k2_kernel(const u16* __restrict__ x, const char* __restrict__ ws,
                                                    u16* __restrict__ out) {
  __shared__ __attribute__((aligned(16))) char red[4 * 20 * 1024];
  const int b = blockIdx.x, xcd = b & 7, idx = b >> 3;
  const int rb = 4 * xcd + (idx >> 3), nb = idx & 7;
  const int lane = threadIdx.x & 63;
  const int wave = __builtin_amdgcn_readfirstlane(threadIdx.x >> 6);
  const char* ap = ws + ((size_t)(rb * 4) * KS2 + 17 * wave) * 1024 + lane * 16;
  const char* wp = ws + WF_OFF + ((size_t)(17 * wave) * 40 + nb * 5) * 1024 + lane * 16;
  constexpr int DEP = 4;
  bf16x8 af[DEP][4], wf[DEP][5];
  f32x4 acc[4][5];
#pragma unroll
  for (int i = 0; i < 4; ++i)
#pragma unroll
    for (int j = 0; j < 5; ++j) acc[i][j] = (f32x4){0.f, 0.f, 0.f, 0.f};
  auto ld = [&](int st, int buf) {
#pragma unroll
    for (int i = 0; i < 4; ++i) af[buf][i] = *(const bf16x8*)(ap + (size_t)i * KS2 * 1024 + st * 1024);
#pragma unroll
    for (int j = 0; j < 5; ++j) wf[buf][j] = *(const bf16x8*)(wp + (size_t)st * 40960 + j * 1024);
  };
#pragma unroll
  for (int st = 0; st < DEP - 1; ++st) ld(st, st);
#pragma unroll
  for (int st = 0; st < 17; ++st) {
    if (st + DEP - 1 < 17) ld(st + DEP - 1, (st + DEP - 1) % DEP);
#pragma unroll
    for (int i = 0; i < 4; ++i)
#pragma unroll
      for (int j = 0; j < 5; ++j)
        acc[i][j] = __builtin_amdgcn_mfma_f32_16x16x32_bf16(wf[st % DEP][j], af[st % DEP][i], acc[i][j], 0, 0, 0);
  }
#pragma unroll
  for (int i = 0; i < 4; ++i)
#pragma unroll
    for (int j = 0; j < 5; ++j) *(f32x4*)(red + ((wave * 20 + i * 5 + j) * 64 + lane) * 16) = acc[i][j];
  __syncthreads();
  const int g = lane >> 4, r = lane & 15;
  const int row = rb * 64 + wave * 16 + r;
#pragma unroll
  for (int j = 0; j < 5; ++j) {
    f32x4 s = *(const f32x4*)(red + ((0 * 20 + wave * 5 + j) * 64 + lane) * 16);
#pragma unroll
    for (int w = 1; w < 4; ++w) s += *(const f32x4*)(red + ((w * 20 + wave * 5 + j) * 64 + lane) * 16);
    const int col = nb * 80 + j * 16 + g * 4;
    u32x2 xv = *(const u32x2*)(x + (size_t)row * D_ + col);
    u32x2 o;
    o[0] = pack2(bflo(xv[0]) + s[0], bfhi(xv[0]) + s[1]);
    o[1] = pack2(bflo(xv[1]) + s[2], bfhi(xv[1]) + s[3]);
    *(u32x2*)(out + (size_t)row * D_ + col) = o;
  }
}

}

extern "C" void kernel_launch(void* const* d_in, const int* in_sizes, int n_in, void* d_out, int out_size,
                              void* d_ws, size_t ws_size, hipStream_t stream) {
  const u16* x = (const u16*)d_in[0];
  const u16* wqkv = (const u16*)d_in[1];
  const u16* wo = (const u16*)d_in[2];
  const u16* w1 = (const u16*)d_in[4];
  const u16* w3 = (const u16*)d_in[5];
  const u16* w2 = (const u16*)d_in[6];
  k1_kernel<<<256, 512, 0, stream>>>(x, wqkv, wo, w1, w3, w2, (char*)d_ws);
  k2_kernel<<<256, 256, 0, stream>>>(x, (const char*)d_ws, (u16*)d_out);
}
```

```cpp
#include <hip/hip_runtime.h>
#include <hip/hip_bf16.h>
#include <stdint.h>

typedef __attribute__((ext_vector_type(8))) short bf16x8;
typedef __attribute__((ext_vector_type(4))) short s16x4;
typedef __attribute__((ext_vector_type(2))) __bf16 bf16x2v;
typedef __attribute__((ext_vector_type(4))) float f32x4;
typedef __attribute__((ext_vector_type(16))) float f32x16;
typedef __attribute__((ext_vector_type(4))) unsigned u32x4;
typedef __attribute__((ext_vector_type(2))) unsigned u32x2;
typedef unsigned short u16;
#define LDS_AS __attribute__((address_space(3)))

#define STAMP(i) do { } while (0)

namespace {

constexpr int S_ = 2048, D_ = 640, F_ = 1536;
constexpr int KS2 = 68;
constexpr size_t ACT_BYTES = (size_t)128 * KS2 * 1024;
constexpr size_t WF_OFF = ACT_BYTES;

constexpr int XSLOT = 24576, NXS = 3;
constexpr int WSLOT = 16384, NWS = 5;
constexpr int WRING = XSLOT * NXS;
constexpr int ONES_OFF = WRING + WSLOT * NWS;
constexpr int K1_LDS = ONES_OFF + 64;
constexpr int P_STAT = 0;
constexpr int P_CS = 12288;
constexpr int P_ROW = 13312;
constexpr int P_QKV = 16384;
constexpr int QKV_STRIDE = 488;

#define WAIT_V(n) asm volatile("s_waitcnt vmcnt(%0)" ::"n"(n) : "memory")
#define WAIT_L0() asm volatile("s_waitcnt lgkmcnt(0)" ::: "memory")
#define RAW_BARRIER() do { WAIT_L0(); __builtin_amdgcn_s_barrier(); } while (0)

__device__ __forceinline__ float bfr(float f) { return (float)(__bf16)f; }
__device__ __forceinline__ unsigned pack2(float a, float b) {
  bf16x2v v; v[0] = (__bf16)a; v[1] = (__bf16)b; return __builtin_bit_cast(unsigned, v);
}
__device__ __forceinline__ float bflo(unsigned u) { return __builtin_bit_cast(float, u << 16); }
__device__ __forceinline__ float bfhi(unsigned u) { return __builtin_bit_cast(float, u & 0xffff0000u); }
__device__ __forceinline__ float dot2(unsigned a, unsigned b, float c) {
  return __builtin_amdgcn_fdot2_f32_bf16(__builtin_bit_cast(bf16x2v, a), __builtin_bit_cast(bf16x2v, b), c, false);
}
__device__ __forceinline__ void vadd(float& a, float b) { asm("v_add_f32 %0, %0, %1" : "+v"(a) : "v"(b)); }
__device__ __forceinline__ void vsq(float& a, float b) { asm("v_fmac_f32 %0, %1, %1" : "+v"(a) : "v"(b)); }
__device__ __forceinline__ void dma16(const void* g, char* lds_uniform) {
  __builtin_amdgcn_global_load_lds((const unsigned*)g, (unsigned*)lds_uniform, 16, 0, 0);
}
__device__ __forceinline__ s16x4 tr_read(const char* p) {
  return __builtin_amdgcn_ds_read_tr16_b64_v4i16((LDS_AS s16x4*)p);
}

#define SB() __builtin_amdgcn_sched_barrier(0)

template <bool QKV, int MTW, int PH>
__device__ __forceinline__ void k1_body(char* smem, const u16* __restrict__ x, const u16* __restrict__ wa,
                                        const u16* __restrict__ wb, char* __restrict__ act, int rb, int cb,
                                        const int wave) {
  constexpr int MT = QKV ? 5 : 6;
  constexpr int BMR = MT * 32;
  char* stampbuf = act + (64u << 20); (void)stampbuf;
  STAMP(0);
  const int tid = threadIdx.x, lane = tid & 63;
  const int wm = wave >> 2, wn = wave & 3;
  const int h = lane >> 5, r32 = lane & 31;
  const int row0 = QKV ? rb * 144 - 16 : rb * 191 - 1;

  const char* xsrc[3];
#pragma unroll
  for (int j = 0; j < 3; ++j) {
    int lr = 8 * (wave + 8 * j) + (lane >> 3);
    int c = (lane & 7) ^ ((lr >> 1) & 7);
    int gr = row0 + lr; gr = gr < 0 ? 0 : (gr > S_ - 1 ? S_ - 1 : gr);
    xsrc[j] = (const char*)x + (size_t)gr * (D_ * 2) + c * 16;
  }
  constexpr bool X3 = !QKV || (MTW == 3);
  const char* wsrc[2];
#pragma unroll
  for (int j = 0; j < 2; ++j) {
    int k = 4 * wave + 2 * j + (lane >> 5);
    int c = (lane & 31) ^ ((k & 3) << 2);
    if (QKV) {
      int cc = c > 29 ? 29 : c;
      int seg = cc / 10, cq = cc - seg * 10;
      wsrc[j] = (const char*)wa + ((size_t)k * 1920 + seg * 640 + cb * 80 + cq * 8) * 2;
    } else {
      int wv = c >> 2, part = (c >> 1) & 1, hf = c & 1;
      wsrc[j] = (const char*)(part ? wb : wa) + ((size_t)k * F_ + cb * 128 + wv * 16 + hf * 8) * 2;
    }
  }
  constexpr int WADV = (QKV ? 1920 : F_) * 2 * 32;

  bool inloop = false; (void)inloop;
  auto dmaW = [&](int j, int slot, int adv) {
    dma16(wsrc[j], smem + WRING + slot * WSLOT + wave * 2048 + j * 1024);
    wsrc[j] += adv;
  };
  auto dmaX = [&](int j, int slot, int adv) {
    if (j < 2 || X3) dma16(xsrc[j], smem + slot * XSLOT + wave * 1024 + j * 8192);
    xsrc[j] += adv;
  };
  auto issueW = [&](int slot) { dmaW(0, slot, WADV); dmaW(1, slot, WADV); };
  auto issueX = [&](int slot) { dmaX(0, slot, 128); dmaX(1, slot, 128); dmaX(2, slot, 128); };

  int xoff[4];
#pragma unroll
  for (int q = 0; q < 4; ++q) xoff[q] = (wm * 96 + r32) * 128 + (((2 * q + h) ^ ((r32 >> 1) & 7)) * 16);
  const bool onesl = (wm == 0) && (r32 == 0);
  int woff0, woff1;
  {
    int blk = (lane >> 4) & 1, q4 = (lane & 15) >> 2, p = lane & 3;
    woff0 = WRING + (8 * h + q4) * 512 + (((8 * wn + 2 * blk + (p >> 1)) ^ (q4 << 2)) * 16) + (p & 1) * 8;
    woff1 = woff0 ^ 64;
  }

  f32x16 acc[MTW][2];
#pragma unroll
  for (int m = 0; m < MTW; ++m)
#pragma unroll
    for (int i = 0; i < 16; ++i) { acc[m][0][i] = 0.f; acc[m][1][i] = 0.f; }

  constexpr int RPW = BMR / 8;
  float sx[3][2], sxx[3][2];
#pragma unroll
  for (int i = 0; i < 3; ++i) { sx[i][0] = sx[i][1] = 0.f; sxx[i][0] = sxx[i][1] = 0.f; }
  const int soff = (RPW * wave + (lane >> 3)) * 128 + (lane & 7) * 16;
  u32x4 sv[3];
  auto stats_ld = [&](int xbase) {
#pragma unroll
    for (int i = 0; i < 3; ++i) sv[i] = *(const u32x4*)(smem + xbase + soff + ((i < 2 || !QKV || lane < 32) ? i * 1024 : 0));
  };
  auto stats_acc = [&](int i, int d0) {
#pragma unroll
    for (int d = d0; d < d0 + 2; ++d) {
      float lo = bflo(sv[i][d]), hi = bfhi(sv[i][d]);
      vadd(sx[i][0], lo); vadd(sx[i][1], hi);
      vsq(sxx[i][0], lo); vsq(sxx[i][1], hi);
    }
  };

  bf16x8 xf0[MTW], xf1[MTW], wf0[2], wf1[2];
  auto LX = [&](bf16x8(&xf)[MTW], int m, int xbase, int q) {
    if (m >= MTW) return;
    const int a = xbase + xoff[q];
    if (m == 0) xf[0] = *(const bf16x8*)(smem + (onesl ? ONES_OFF : a));
    else xf[m] = *(const bf16x8*)(smem + a + m * 4096);
  };
  auto LW = [&](bf16x8(&wf)[2], int j, int wbase, int k16) {
    const int o = wbase + (j ? woff1 : woff0) + k16 * 8192;
    s16x4 a0 = tr_read(smem + o);
    s16x4 a1 = tr_read(smem + o + 2048);
    wf[j] = __builtin_shufflevector(a0, a1, 0, 1, 2, 3, 4, 5, 6, 7);
  };
  auto MF = [&](int i, bf16x8(&xf)[MTW], bf16x8(&wf)[2]) {
    SB();
    if ((i >> 1) < MTW)
      acc[i >> 1][i & 1] = __builtin_amdgcn_mfma_f32_32x32x16_bf16(wf[i & 1], xf[i >> 1], acc[i >> 1][i & 1], 0, 0, 0);
    SB();
  };

  if (tid == 0) *(u32x4*)(smem + ONES_OFF) = (u32x4){0x3f803f80u, 0x3f803f80u, 0x3f803f80u, 0x3f803f80u};
  issueW(0); issueX(0); issueW(1); issueW(2); issueX(1); issueW(3);
  WAIT_V(X3 ? 9 : 8);
  RAW_BARRIER();
  STAMP(1);
  LX(xf0, 0, 0, 0); LX(xf0, 1, 0, 0); LX(xf0, 2, 0, 0); LW(wf0, 0, 0, 0); LW(wf0, 1, 0, 0);

  inloop = true;
  auto sub = [&](bf16x8(&xc)[MTW], bf16x8(&wc)[2], bf16x8(&xn)[MTW], bf16x8(&wn2)[2], int xbn, int qn, int wbn,
                 int k16n, auto extra) {
    MF(0, xc, wc); LW(wn2, 0, wbn, k16n); extra(0);
    MF(1, xc, wc); LW(wn2, 1, wbn, k16n); LX(xn, 0, xbn, qn); extra(1);
    MF(2, xc, wc); LX(xn, 1, xbn, qn); LX(xn, 2, xbn, qn); extra(2);
    MF(3, xc, wc); extra(3);
    MF(4, xc, wc); extra(4);
    MF(5, xc, wc); extra(5);
    SB();
  };
  int ws0 = 0;
  int xs0 = 0;
  for (int s = 0; s < 20; s += 2) {
    const int wadv = s < 15 ? WADV : 0;
    const int xadv = s < 14 ? 128 : 0;
    const int wadv2 = s < 14 ? WADV : 0;
    {
      WAIT_V(X3 ? 7 : 6);
      RAW_BARRIER();
      int ws4 = ws0 + 4; if (ws4 >= NWS) ws4 -= NWS;
      int xs2 = xs0 + 2; if (xs2 >= NXS) xs2 -= NXS;
      int ws1 = ws0 + 1; if (ws1 >= NWS) ws1 -= NWS;
      const int xb = xs0 * XSLOT, wb0 = ws0 * WSLOT, wb1 = ws1 * WSLOT;
      auto heavy = [&](int i) {
        if (i == 1) dmaW(0, ws4, wadv);
        if (i == 2) dmaW(1, ws4, wadv);
        if (i == 3) dmaX(0, xs2, xadv);
        if (i == 4) dmaX(1, xs2, xadv);
        if (i == 5) dmaX(2, xs2, xadv);
      };
      auto light = [&](int i) { if (PH == 0 && i == 3) stats_ld(xb); };
      if (PH == 0) {
        sub(xf0, wf0, xf1, wf1, xb, 1, wb0, 1, heavy);
        sub(xf1, wf1, xf0, wf0, xb, 2, wb1, 0, light);
      } else {
        sub(xf0, wf0, xf1, wf1, xb, 1, wb0, 1, light);
        sub(xf1, wf1, xf0, wf0, xb, 2, wb1, 0, heavy);
      }
      ws0 = ws1;
    }
    {
      WAIT_V(X3 ? 7 : 6);
      RAW_BARRIER();
      int ws4 = ws0 + 4; if (ws4 >= NWS) ws4 -= NWS;
      int ws1 = ws0 + 1; if (ws1 >= NWS) ws1 -= NWS;
      int xs1 = xs0 + 1; if (xs1 >= NXS) xs1 -= NXS;
      const int xb = xs0 * XSLOT, xb1 = xs1 * XSLOT, wb0 = ws0 * WSLOT, wb1 = ws1 * WSLOT;
      auto heavy = [&](int i) {
        stats_acc(i >> 1, (i & 1) * 2);
        if (i == 3) dmaW(0, ws4, wadv2);
        if (i == 5) dmaW(1, ws4, wadv2);
      };
      auto light = [&](int i) { if (PH == 1 && i == 3) stats_ld(xb); };
      if (PH == 0) {
        sub(xf0, wf0, xf1, wf1, xb, 3, wb0, 1, heavy);
        sub(xf1, wf1, xf0, wf0, xb1, 0, wb1, 0, light);
      } else {
        sub(xf0, wf0, xf1, wf1, xb, 3, wb0, 1, light);
        sub(xf1, wf1, xf0, wf0, xb1, 0, wb1, 0, heavy);
      }
      ws0 = ws1; xs0 = xs1;
    }
  }
  WAIT_V(0);

  RAW_BARRIER();
  STAMP(2);
#pragma unroll
  for (int i = 0; i < 3; ++i) {
    float a = sx[i][0] + sx[i][1], b = sxx[i][0] + sxx[i][1];
    a += __shfl_xor(a, 1); b += __shfl_xor(b, 1);
    a += __shfl_xor(a, 2); b += __shfl_xor(b, 2);
    a += __shfl_xor(a, 4); b += __shfl_xor(b, 4);
    float mean = a * (1.f / D_);
    float var = b * (1.f / D_) - mean * mean; var = var < 0.f ? 0.f : var;
    float mb = bfr(mean);
    float den = bfr(sqrtf(bfr(bfr(var) + 1e-5f)));
    if ((lane & 7) == 0 && (i < 2 || !QKV || lane < 32))
      ((float2*)(smem + P_ROW))[RPW * wave + (lane >> 3) + 8 * i] = make_float2(mb, 1.f / den);
  }
  if (onesl) {
#pragma unroll
    for (int j = 0; j < 2; ++j)
#pragma unroll
      for (int q = 0; q < 4; ++q)
        *(f32x4*)(smem + P_CS + ((2 * wn + j) * 32 + 8 * q + 4 * h) * 4) =
            (f32x4){acc[0][j][4 * q], acc[0][j][4 * q + 1], acc[0][j][4 * q + 2], acc[0][j][4 * q + 3]};
  }
  __syncthreads();
  STAMP(3);

  float2 rs[MTW];
#pragma unroll
  for (int m = 0; m < MTW; ++m) rs[m] = ((const float2*)(smem + P_ROW))[(wm * 3 + m) * 32 + r32];

  if (!QKV) {
    const int ks = 20 + cb * 4 + wn;
#pragma unroll
    for (int j = 0; j < 2; ++j) {
      float csv[16];
#pragma unroll
      for (int q = 0; q < 4; ++q) {
        f32x4 v = *(const f32x4*)(smem + P_CS + ((2 * wn + j) * 32 + 8 * q + 4 * h) * 4);
        csv[4 * q] = v[0]; csv[4 * q + 1] = v[1]; csv[4 * q + 2] = v[2]; csv[4 * q + 3] = v[3];
      }
#pragma unroll
      for (int m = 0; m < MTW; ++m) {
        float hv[8];
#pragma unroll
        for (int i = 0; i < 8; ++i) {
          float g = bfr((acc[m][j][i] - rs[m].x * csv[i]) * rs[m].y);
          float u = bfr((acc[m][j][8 + i] - rs[m].x * csv[8 + i]) * rs[m].y);
          float sg = bfr(g * __builtin_amdgcn_rcpf(1.f + __expf(-g)));
          hv[i] = sg * u;
        }
        int grow = row0 + (wm * 3 + m) * 32 + r32;
        if (grow < S_ && !(m == 0 && onesl)) {
          u32x4 o; o[0] = pack2(hv[0], hv[1]); o[1] = pack2(hv[2], hv[3]); o[2] = pack2(hv[4], hv[5]); o[3] = pack2(hv[6], hv[7]);
          *(u32x4*)(act + ((size_t)(grow >> 4) * KS2 + ks) * 1024 + ((2 * j + h) * 16 + (grow & 15)) * 16) = o;
        }
      }
    }
  } else {
#pragma unroll
    for (int j = 0; j < 2; ++j) {
#pragma unroll
      for (int q = 0; q < 4; ++q) {
        const int col = (2 * wn + j) * 32 + 8 * q + 4 * h;
        f32x4 cv = *(const f32x4*)(smem + P_CS + col * 4);
#pragma unroll
        for (int m = 0; m < MTW; ++m) {
          float v0 = (acc[m][j][4 * q] - rs[m].x * cv[0]) * rs[m].y, v1 = (acc[m][j][4 * q + 1] - rs[m].x * cv[1]) * rs[m].y;
          float v2 = (acc[m][j][4 * q + 2] - rs[m].x * cv[2]) * rs[m].y, v3 = (acc[m][j][4 * q + 3] - rs[m].x * cv[3]) * rs[m].y;
          u32x2 o; o[0] = pack2(v0, v1); o[1] = pack2(v2, v3);
          if (col < 240) *(u32x2*)(smem + P_QKV + ((wm * 3 + m) * 32 + r32) * QKV_STRIDE + col * 2) = o;
        }
      }
    }
    __syncthreads();
    STAMP(4);
    const float scale = 0.22360679774997896f;
    for (int pi = tid; pi < 576; pi += 512) {
      int hl = pi / 144, rl = pi - hl * 144;
      int s = rb * 144 + rl;
      if (s >= S_) continue;
      const char* qrow = smem + P_QKV + (16 + rl) * QKV_STRIDE + hl * 40;
      unsigned qv[10];
#pragma unroll
      for (int i = 0; i < 5; ++i) { u32x2 t = *(const u32x2*)(qrow + i * 8); qv[2 * i] = t[0]; qv[2 * i + 1] = t[1]; }
      float lg[8];
#pragma unroll
      for (int w = 0; w < 8; ++w) {
        const char* krow = qrow - 2 * w * QKV_STRIDE + 160;
        float d = 0.f;
#pragma unroll
        for (int i = 0; i < 5; ++i) { u32x2 t = *(const u32x2*)(krow + i * 8); d = dot2(qv[2 * i], t[0], d); d = dot2(qv[2 * i + 1], t[1], d); }
        lg[w] = (s - 2 * w >= 0) ? bfr(bfr(d) * scale) : -1e9f;
      }
      float mx = lg[0];
#pragma unroll
      for (int w = 1; w < 8; ++w) mx = fmaxf(mx, lg[w]);
      float sum = 0.f;
#pragma unroll
      for (int w = 0; w < 8; ++w) { lg[w] = __expf(lg[w] - mx); sum += lg[w]; }
      float inv = __builtin_amdgcn_rcpf(sum);
      float o[20];
#pragma unroll
      for (int d = 0; d < 20; ++d) o[d] = 0.f;
#pragma unroll
      for (int w = 0; w < 8; ++w) {
        float p = bfr(lg[w] * inv);
        const char* vrow = qrow - 2 * w * QKV_STRIDE + 320;
#pragma unroll
        for (int i = 0; i < 5; ++i) {
          u32x2 t = *(const u32x2*)(vrow + i * 8);
          o[4 * i] += p * bflo(t[0]); o[4 * i + 1] += p * bfhi(t[0]);
          o[4 * i + 2] += p * bflo(t[1]); o[4 * i + 3] += p * bfhi(t[1]);
        }
      }
      char* obase = act + (size_t)(s >> 4) * KS2 * 1024 + (s & 15) * 16;
#pragma unroll
      for (int i = 0; i < 5; ++i) {
        int col = cb * 80 + hl * 20 + 4 * i;
        int ks = col >> 5, kk = col & 31;
        int g = 2 * (kk >> 4) + ((kk >> 2) & 1), jh = (kk >> 3) & 1;
        u32x2 t; t[0] = pack2(o[4 * i], o[4 * i + 1]); t[1] = pack2(o[4 * i + 2], o[4 * i + 3]);
        *(u32x2*)(obase + ks * 1024 + g * 256 + jh * 8) = t;
      }
    }
  }
  STAMP(5);
}

__global__ __launch_bounds__(512, 2) void k1_kernel(const u16* __restrict__ x, const u16* __restrict__ wqkv,
                                                    const u16* __restrict__ wo, const u16* __restrict__ w1,
                                                    const u16* __restrict__ w3, const u16* __restrict__ w2,
                                                    char* __restrict__ ws) {
  __shared__ __attribute__((aligned(16))) char smem[K1_LDS];
  const int b = blockIdx.x, xcd = b & 7, idx = b >> 3;
  const int wave = __builtin_amdgcn_readfirstlane(threadIdx.x >> 6);
  if (idx < 15) {
    if (wave < 4) k1_body<true, 3, 0>(smem, x, wqkv, wqkv, ws, idx, xcd, wave);
    else k1_body<true, 2, 1>(smem, x, wqkv, wqkv, ws, idx, xcd, wave);
  } else {
    const int t = xcd * 17 + (idx - 15);
    if (t >= 132) return;
    if (wave < 4) k1_body<false, 3, 0>(smem, x, w1, w3, ws, t % 11, t / 11, wave);
    else k1_body<false, 3, 1>(smem, x, w1, w3, ws, t % 11, t / 11, wave);
    char* wf = ws + WF_OFF;
    for (int sl = t * 512 + threadIdx.x; sl < KS2 * 40 * 64; sl += 132 * 512) {
      int frag = sl >> 6, l = sl & 63;
      int ks = frag / 40, nt = frag - ks * 40;
      int g = l >> 4, c = l & 15;
      int k0 = 32 * ks + 16 * (g >> 1) + 4 * (g & 1);
      const u16* src = (ks < 20 ? wo + (size_t)k0 * D_ : w2 + (size_t)(k0 - D_) * D_) + nt * 16 + c;
      unsigned e[8];
#pragma unroll
      for (int j = 0; j < 8; ++j) e[j] = src[(size_t)(8 * (j >> 2) + (j & 3)) * D_];
      u32x4 o; o[0] = e[0] | (e[1] << 16); o[1] = e[2] | (e[3] << 16); o[2] = e[4] | (e[5] << 16); o[3] = e[6] | (e[7] << 16);
      *(u32x4*)(wf + (size_t)sl * 16) = o;
    }
    { char* stampbuf = ws + (64u << 20); (void)stampbuf; STAMP(6); }
  }
}

__global__ __launch_bounds__(256, 1) void k2_kernel(const u16* __restrict__ x, const char* __restrict__ ws,
                                                    u16* __restrict__ out) {
  __shared__ __attribute__((aligned(16))) char red[4 * 20 * 1024];
  const int b = blockIdx.x, xcd = b & 7, idx = b >> 3;
  const int rb = 4 * xcd + (idx >> 3), nb = idx & 7;
  STAMP(0);
  const int lane = threadIdx.x & 63;
  const int wave = __builtin_amdgcn_readfirstlane(threadIdx.x >> 6);
  const char* ap = ws + ((size_t)(rb * 4) * KS2 + 17 * wave) * 1024 + lane * 16;
  const char* wp = ws + WF_OFF + ((size_t)(17 * wave) * 40 + nb * 5) * 1024 + lane * 16;
  constexpr int DEP = 6;
  bf16x8 af[DEP][4], wf[DEP][5];
  f32x4 acc[4][5];
#pragma unroll
  for (int i = 0; i < 4; ++i)
#pragma unroll
    for (int j = 0; j < 5; ++j) acc[i][j] = (f32x4){0.f, 0.f, 0.f, 0.f};
  auto ld = [&](int st, int buf) {
#pragma unroll
    for (int i = 0; i < 4; ++i) af[buf][i] = *(const bf16x8*)(ap + (size_t)i * KS2 * 1024 + st * 1024);
#pragma unroll
    for (int j = 0; j < 5; ++j) wf[buf][j] = *(const bf16x8*)(wp + (size_t)st * 40960 + j * 1024);
  };
#pragma unroll
  for (int st = 0; st < DEP - 1; ++st) ld(st, st);
#pragma unroll
  for (int st = 0; st < 17; ++st) {
    if (st + DEP - 1 < 17) ld(st + DEP - 1, (st + DEP - 1) % DEP);
    __builtin_amdgcn_sched_barrier(0);
#pragma unroll
    for (int i = 0; i < 4; ++i)
#pragma unroll
      for (int j = 0; j < 5; ++j)
        acc[i][j] = __builtin_amdgcn_mfma_f32_16x16x32_bf16(wf[st % DEP][j], af[st % DEP][i], acc[i][j], 0, 0, 0);
    __builtin_amdgcn_sched_barrier(0);
  }
  STAMP(2);
#pragma unroll
  for (int i = 0; i < 4; ++i)
#pragma unroll
    for (int j = 0; j < 5; ++j) *(f32x4*)(red + ((wave * 20 + i * 5 + j) * 64 + lane) * 16) = acc[i][j];
  __syncthreads();
  const int g = lane >> 4, r = lane & 15;
  const int row = rb * 64 + wave * 16 + r;
#pragma unroll
  for (int j = 0; j < 5; ++j) {
    f32x4 s = *(const f32x4*)(red + ((0 * 20 + wave * 5 + j) * 64 + lane) * 16);
#pragma unroll
    for (int w = 1; w < 4; ++w) s += *(const f32x4*)(red + ((w * 20 + wave * 5 + j) * 64 + lane) * 16);
    const int col = nb * 80 + j * 16 + g * 4;
    u32x2 xv = *(const u32x2*)(x + (size_t)row * D_ + col);
    u32x2 o;
    o[0] = pack2(bflo(xv[0]) + s[0], bfhi(xv[0]) + s[1]);
    o[1] = pack2(bflo(xv[1]) + s[2], bfhi(xv[1]) + s[3]);
    *(u32x2*)(out + (size_t)row * D_ + col) = o;
  }
  STAMP(3);
}

}

extern "C" void kernel_launch(void* const* d_in, const int* in_sizes, int n_in, void* d_out, int out_size,
                              void* d_ws, size_t ws_size, hipStream_t stream) {
  const u16* x = (const u16*)d_in[0];
  const u16* wqkv = (const u16*)d_in[1];
  const u16* wo = (const u16*)d_in[2];
  const u16* w1 = (const u16*)d_in[4];
  const u16* w3 = (const u16*)d_in[5];
  const u16* w2 = (const u16*)d_in[6];
  k1_kernel<<<256, 512, 0, stream>>>(x, wqkv, wo, w1, w3, w2, (char*)d_ws);
  k2_kernel<<<256, 256, 0, stream>>>(x, (const char*)d_ws, (u16*)d_out);
}
```

```cpp
#include <hip/hip_runtime.h>
#include <hip/hip_bf16.h>
#include <stdint.h>
#include <type_traits>

typedef __attribute__((ext_vector_type(8))) short bf16x8;
typedef __attribute__((ext_vector_type(4))) short s16x4;
typedef __attribute__((ext_vector_type(2))) __bf16 bf16x2v;
typedef __attribute__((ext_vector_type(4))) float f32x4;
typedef __attribute__((ext_vector_type(16))) float f32x16;
typedef __attribute__((ext_vector_type(4))) unsigned u32x4;
typedef __attribute__((ext_vector_type(2))) unsigned u32x2;
typedef unsigned short u16;
#define LDS_AS __attribute__((address_space(3)))

#define STAMP(i) do { } while (0)

namespace {

constexpr int S_ = 2048, D_ = 640, F_ = 1536;
constexpr int KS2 = 68;
constexpr size_t ACT_BYTES = (size_t)128 * KS2 * 1024;
constexpr size_t WF_OFF = ACT_BYTES;

constexpr int XSLOT = 24576, NXS = 3;
constexpr int WSLOT = 16384, NWS = 5;
constexpr int WRING = XSLOT * NXS;
constexpr int ONES_OFF = WRING + WSLOT * NWS;
constexpr int K1_LDS = ONES_OFF + 64;
constexpr int P_STAT = 0;
constexpr int P_CS = 12288;
constexpr int P_ROW = 13312;
constexpr int P_QKV = 16384;
constexpr int QKV_STRIDE = 488;

#define WAIT_V(n) asm volatile("s_waitcnt vmcnt(%0)" ::"n"(n) : "memory")
#define WAIT_L0() asm volatile("s_waitcnt lgkmcnt(0)" ::: "memory")
#define RAW_BARRIER() do { WAIT_L0(); __builtin_amdgcn_s_barrier(); } while (0)

__device__ __forceinline__ float bfr(float f) { return (float)(__bf16)f; }
__device__ __forceinline__ unsigned pack2(float a, float b) {
  bf16x2v v; v[0] = (__bf16)a; v[1] = (__bf16)b; return __builtin_bit_cast(unsigned, v);
}
__device__ __forceinline__ float bflo(unsigned u) { return __builtin_bit_cast(float, u << 16); }
__device__ __forceinline__ float bfhi(unsigned u) { return __builtin_bit_cast(float, u & 0xffff0000u); }
__device__ __forceinline__ float dot2(unsigned a, unsigned b, float c) {
  return __builtin_amdgcn_fdot2_f32_bf16(__builtin_bit_cast(bf16x2v, a), __builtin_bit_cast(bf16x2v, b), c, false);
}
__device__ __forceinline__ void vadd(float& a, float b) { asm("v_add_f32 %0, %0, %1" : "+v"(a) : "v"(b)); }
__device__ __forceinline__ void vsq(float& a, float b) { asm("v_fmac_f32 %0, %1, %1" : "+v"(a) : "v"(b)); }
__device__ __forceinline__ void st16(void* p, u32x4 v) {
  asm volatile("global_store_dwordx4 %0, %1, off sc1\n\ts_nop 1" ::"v"(p), "v"(v) : "memory");
}
__device__ __forceinline__ void st8(void* p, u32x2 v) {
  asm volatile("global_store_dwordx2 %0, %1, off sc1\n\ts_nop 1" ::"v"(p), "v"(v) : "memory");
}
__device__ __forceinline__ void dma16(const void* g, char* lds_uniform) {
  __builtin_amdgcn_global_load_lds((const unsigned*)g, (unsigned*)lds_uniform, 16, 0, 0);
}
__device__ __forceinline__ s16x4 tr_read(const char* p) {
  return __builtin_amdgcn_ds_read_tr16_b64_v4i16((LDS_AS s16x4*)p);
}

#define SB() __builtin_amdgcn_sched_barrier(0)

template <bool QKV, int MTW, int PH>
__device__ __forceinline__ void k1_body(char* smem, const u16* __restrict__ x, const u16* __restrict__ wa,
                                        const u16* __restrict__ wb, char* __restrict__ act, int rb, int cb,
                                        const int wave, const u16* __restrict__ wo, const u16* __restrict__ w2,
                                        int t) {
  constexpr int MT = QKV ? 5 : 6;
  constexpr int BMR = MT * 32;
  char* stampbuf = act + (64u << 20); (void)stampbuf;
  STAMP(0);
  const int tid = threadIdx.x, lane = tid & 63;
  const int wm = wave >> 2, wn = wave & 3;
  const int h = lane >> 5, r32 = lane & 31;
  const int row0 = QKV ? rb * 144 - 16 : rb * 191 - 1;

  const char* xsrc[3];
#pragma unroll
  for (int j = 0; j < 3; ++j) {
    int lr = 8 * (wave + 8 * j) + (lane >> 3);
    int c = (lane & 7) ^ ((lr >> 1) & 7);
    int gr = row0 + lr; gr = gr < 0 ? 0 : (gr > S_ - 1 ? S_ - 1 : gr);
    xsrc[j] = (const char*)x + (size_t)gr * (D_ * 2) + c * 16;
  }
  constexpr bool X3 = !QKV || (MTW == 3);
  const char* wsrc[2];
#pragma unroll
  for (int j = 0; j < 2; ++j) {
    int k = 4 * wave + 2 * j + (lane >> 5);
    int c = (lane & 31) ^ ((k & 3) << 2);
    if (QKV) {
      int cc = c > 29 ? 29 : c;
      int seg = cc / 10, cq = cc - seg * 10;
      wsrc[j] = (const char*)wa + ((size_t)k * 1920 + seg * 640 + cb * 80 + cq * 8) * 2;
    } else {
      int wv = c >> 2, part = (c >> 1) & 1, hf = c & 1;
      wsrc[j] = (const char*)(part ? wb : wa) + ((size_t)k * F_ + cb * 128 + wv * 16 + hf * 8) * 2;
    }
  }
  constexpr int WADV = (QKV ? 1920 : F_) * 2 * 32;

  bool inloop = false; (void)inloop;
  auto dmaW = [&](int j, int slot, int adv) {
    dma16(wsrc[j], smem + WRING + slot * WSLOT + wave * 2048 + j * 1024);
    wsrc[j] += adv;
  };
  auto dmaX = [&](int j, int slot, int adv) {
    if (j < 2 || X3) dma16(xsrc[j], smem + slot * XSLOT + wave * 1024 + j * 8192);
    xsrc[j] += adv;
  };
  auto issueW = [&](int slot) { dmaW(0, slot, WADV); dmaW(1, slot, WADV); };
  auto issueX = [&](int slot) { dmaX(0, slot, 128); dmaX(1, slot, 128); dmaX(2, slot, 128); };

  int xoff[4];
#pragma unroll
  for (int q = 0; q < 4; ++q) xoff[q] = (wm * 96 + r32) * 128 + (((2 * q + h) ^ ((r32 >> 1) & 7)) * 16);
  const bool onesl = (wm == 0) && (r32 == 0);
  int woff0, woff1;
  {
    int blk = (lane >> 4) & 1, q4 = (lane & 15) >> 2, p = lane & 3;
    woff0 = WRING + (8 * h + q4) * 512 + (((8 * wn + 2 * blk + (p >> 1)) ^ (q4 << 2)) * 16) + (p & 1) * 8;
    woff1 = woff0 ^ 64;
  }

  f32x16 acc[MTW][2];
#pragma unroll
  for (int m = 0; m < MTW; ++m)
#pragma unroll
    for (int i = 0; i < 16; ++i) { acc[m][0][i] = 0.f; acc[m][1][i] = 0.f; }

  constexpr int RPW = BMR / 8;
  float sx[3][2], sxx[3][2];
#pragma unroll
  for (int i = 0; i < 3; ++i) { sx[i][0] = sx[i][1] = 0.f; sxx[i][0] = sxx[i][1] = 0.f; }
  const int soff = (RPW * wave + (lane >> 3)) * 128 + (lane & 7) * 16;
  u32x4 sv[3];
  auto stats_ld = [&](int xbase) {
#pragma unroll
    for (int i = 0; i < 3; ++i) sv[i] = *(const u32x4*)(smem + xbase + soff + ((i < 2 || !QKV || lane < 32) ? i * 1024 : 0));
  };
  auto stats_acc = [&](int i, int d0) {
#pragma unroll
    for (int d = d0; d < d0 + 2; ++d) {
      float lo = bflo(sv[i][d]), hi = bfhi(sv[i][d]);
      vadd(sx[i][0], lo); vadd(sx[i][1], hi);
      vsq(sxx[i][0], lo); vsq(sxx[i][1], hi);
    }
  };

  bf16x8 xf0[MTW], xf1[MTW], wf0[2], wf1[2];
  auto LX = [&](bf16x8(&xf)[MTW], int m, int xbase, int q) {
    if (m >= MTW) return;
    const int a = xbase + xoff[q];
    if (m == 0) xf[0] = *(const bf16x8*)(smem + (onesl ? ONES_OFF : a));
    else xf[m] = *(const bf16x8*)(smem + a + m * 4096);
  };
  auto LW = [&](bf16x8(&wf)[2], int j, int wbase, int k16) {
    const int o = wbase + (j ? woff1 : woff0) + k16 * 8192;
    s16x4 a0 = tr_read(smem + o);
    s16x4 a1 = tr_read(smem + o + 2048);
    wf[j] = __builtin_shufflevector(a0, a1, 0, 1, 2, 3, 4, 5, 6, 7);
  };
  auto MF = [&](int i, bf16x8(&xf)[MTW], bf16x8(&wf)[2]) {
    SB();
    if ((i >> 1) < MTW)
      acc[i >> 1][i & 1] = __builtin_amdgcn_mfma_f32_32x32x16_bf16(wf[i & 1], xf[i >> 1], acc[i >> 1][i & 1], 0, 0, 0);
    SB();
  };

  const int item = t * 165 + tid;
  const bool doit = !QKV && tid < 165 && item < KS2 * 4 * 80;
  u32x4 r[8];
  if (!QKV) {
    const int it2 = doit ? item : 0;
    const int c8 = it2 % 80, kg = it2 / 80, g = kg & 3, ks = kg >> 2;
    const int k0 = 32 * ks + 16 * (g >> 1) + 4 * (g & 1);
    const u16* src = (ks < 20 ? wo + (size_t)k0 * D_ : w2 + (size_t)(k0 - D_) * D_) + c8 * 8;
#pragma unroll
    for (int j = 0; j < 8; ++j) r[j] = *(const u32x4*)(src + (size_t)(8 * (j >> 2) + (j & 3)) * D_);
  }

  if (tid == 0) *(u32x4*)(smem + ONES_OFF) = (u32x4){0x3f803f80u, 0x3f803f80u, 0x3f803f80u, 0x3f803f80u};
  issueW(0); issueX(0); issueW(1); issueW(2); issueX(1); issueW(3);
  WAIT_V(X3 ? 9 : 8);
  RAW_BARRIER();
  STAMP(1);
  LX(xf0, 0, 0, 0); LX(xf0, 1, 0, 0); LX(xf0, 2, 0, 0); LW(wf0, 0, 0, 0); LW(wf0, 1, 0, 0);

  inloop = true;
  auto sub = [&](bf16x8(&xc)[MTW], bf16x8(&wc)[2], bf16x8(&xn)[MTW], bf16x8(&wn2)[2], int xbn, int qn, int wbn,
                 int k16n, auto extra) {
    MF(0, xc, wc); LW(wn2, 0, wbn, k16n); extra(0);
    MF(1, xc, wc); LW(wn2, 1, wbn, k16n); LX(xn, 0, xbn, qn); extra(1);
    MF(2, xc, wc); LX(xn, 1, xbn, qn); LX(xn, 2, xbn, qn); extra(2);
    MF(3, xc, wc); extra(3);
    MF(4, xc, wc); extra(4);
    MF(5, xc, wc); extra(5);
    SB();
  };
  unsigned long long tw = 0, tb = 0; (void)tw; (void)tb;
  int ws0 = 0;
  int xs0 = 0;
  auto pair_steps = [&](auto tailc, bool lastx) {
    constexpr int TAIL = decltype(tailc)::value;
    {
      if (TAIL < 2) { WAIT_V(X3 ? 7 : 6); } else { WAIT_V(0); }
      RAW_BARRIER();
      int ws4 = ws0 + 4; if (ws4 >= NWS) ws4 -= NWS;
      int xs2 = xs0 + 2; if (xs2 >= NXS) xs2 -= NXS;
      int ws1 = ws0 + 1; if (ws1 >= NWS) ws1 -= NWS;
      const int xb = xs0 * XSLOT, wb0 = ws0 * WSLOT, wb1 = ws1 * WSLOT;
      auto heavy = [&](int i) {
        if (TAIL) return;
        if (i == 1) dmaW(0, ws4, WADV);
        if (i == 2) dmaW(1, ws4, WADV);
        if (i == 3) dmaX(0, xs2, 128);
        if (i == 4) dmaX(1, xs2, 128);
        if (i == 5) dmaX(2, xs2, 128);
      };
      auto light = [&](int i) { if (PH == 0 && i == 3) stats_ld(xb); };
      if (PH == 0) {
        sub(xf0, wf0, xf1, wf1, xb, 1, wb0, 1, heavy);
        sub(xf1, wf1, xf0, wf0, xb, 2, wb1, 0, light);
      } else {
        sub(xf0, wf0, xf1, wf1, xb, 1, wb0, 1, light);
        sub(xf1, wf1, xf0, wf0, xb, 2, wb1, 0, heavy);
      }
      ws0 = ws1;
    }
    {
      if (TAIL == 0) { WAIT_V(X3 ? 7 : 6); RAW_BARRIER(); }
      if (TAIL == 1) { WAIT_V(2); RAW_BARRIER(); }
      int ws4 = ws0 + 4; if (ws4 >= NWS) ws4 -= NWS;
      int ws1 = ws0 + 1; if (ws1 >= NWS) ws1 -= NWS;
      int xs1 = xs0 + 1; if (xs1 >= NXS) xs1 -= NXS;
      const int xb = xs0 * XSLOT, xb1 = xs1 * XSLOT, wb0 = ws0 * WSLOT, wb1 = ws1 * WSLOT;
      auto heavy = [&](int i) {
        stats_acc(i >> 1, (i & 1) * 2);
        if (TAIL) return;
        if (i == 3) dmaW(0, ws4, WADV);
        if (i == 5) dmaW(1, ws4, WADV);
      };
      auto light = [&](int i) { if (PH == 1 && i == 3) stats_ld(xb); };
      if (PH == 0) {
        sub(xf0, wf0, xf1, wf1, xb, 3, wb0, 1, heavy);
        sub(xf1, wf1, xf0, wf0, xb1, 0, wb1, 0, light);
      } else {
        sub(xf0, wf0, xf1, wf1, xb, 3, wb0, 1, light);
        sub(xf1, wf1, xf0, wf0, xb1, 0, wb1, 0, heavy);
      }
      ws0 = ws1; xs0 = xs1;
    }
  };
  for (int s = 0; s < 16; s += 2) pair_steps(std::integral_constant<int, 0>{}, false);
  pair_steps(std::integral_constant<int, 1>{}, false);
  pair_steps(std::integral_constant<int, 2>{}, false);

  RAW_BARRIER();
  STAMP(2);
#pragma unroll
  for (int i = 0; i < 3; ++i) {
    float a = sx[i][0] + sx[i][1], b = sxx[i][0] + sxx[i][1];
    a += __shfl_xor(a, 1); b += __shfl_xor(b, 1);
    a += __shfl_xor(a, 2); b += __shfl_xor(b, 2);
    a += __shfl_xor(a, 4); b += __shfl_xor(b, 4);
    float mean = a * (1.f / D_);
    float var = b * (1.f / D_) - mean * mean; var = var < 0.f ? 0.f : var;
    float mb = bfr(mean);
    float den = bfr(sqrtf(bfr(bfr(var) + 1e-5f)));
    if ((lane & 7) == 0 && (i < 2 || !QKV || lane < 32))
      ((float2*)(smem + P_ROW))[RPW * wave + (lane >> 3) + 8 * i] = make_float2(mb, 1.f / den);
  }
  if (onesl) {
#pragma unroll
    for (int j = 0; j < 2; ++j)
#pragma unroll
      for (int q = 0; q < 4; ++q)
        *(f32x4*)(smem + P_CS + ((2 * wn + j) * 32 + 8 * q + 4 * h) * 4) =
            (f32x4){acc[0][j][4 * q], acc[0][j][4 * q + 1], acc[0][j][4 * q + 2], acc[0][j][4 * q + 3]};
  }
  __syncthreads();
  STAMP(3);

  float2 rs[MTW];
#pragma unroll
  for (int m = 0; m < MTW; ++m) rs[m] = ((const float2*)(smem + P_ROW))[(wm * 3 + m) * 32 + r32];

  if (!QKV) {
    const int ks = 20 + cb * 4 + wn;
#pragma unroll
    for (int j = 0; j < 2; ++j) {
      float csv[16];
#pragma unroll
      for (int q = 0; q < 4; ++q) {
        f32x4 v = *(const f32x4*)(smem + P_CS + ((2 * wn + j) * 32 + 8 * q + 4 * h) * 4);
        csv[4 * q] = v[0]; csv[4 * q + 1] = v[1]; csv[4 * q + 2] = v[2]; csv[4 * q + 3] = v[3];
      }
#pragma unroll
      for (int m = 0; m < MTW; ++m) {
        float hv[8];
#pragma unroll
        for (int i = 0; i < 8; ++i) {
          float g = (acc[m][j][i] - rs[m].x * csv[i]) * rs[m].y;
          float u = (acc[m][j][8 + i] - rs[m].x * csv[8 + i]) * rs[m].y;
          hv[i] = g * __builtin_amdgcn_rcpf(1.f + __expf(-g)) * u;
        }
        int grow = row0 + (wm * 3 + m) * 32 + r32;
        if (grow < S_ && !(m == 0 && onesl)) {
          u32x4 o; o[0] = pack2(hv[0], hv[1]); o[1] = pack2(hv[2], hv[3]); o[2] = pack2(hv[4], hv[5]); o[3] = pack2(hv[6], hv[7]);
          st16(act + ((size_t)(grow >> 4) * KS2 + ks) * 1024 + ((2 * j + h) * 16 + (grow & 15)) * 16, o);
        }
      }
    }
    if (doit) {
      const int c8 = item % 80, kg = item / 80, g = kg & 3, ks2 = kg >> 2;
      char* dst = act + WF_OFF + ((size_t)(ks2 * 40 + (c8 >> 1)) * 64 + g * 16 + (c8 & 1) * 8) * 16;
#pragma unroll
      for (int c = 0; c < 8; ++c) {
        u32x4 o;
#pragma unroll
        for (int d = 0; d < 4; ++d)
          o[d] = __builtin_amdgcn_perm(r[2 * d + 1][c >> 1], r[2 * d][c >> 1], (c & 1) ? 0x07060302u : 0x05040100u);
        *(u32x4*)(dst + c * 16) = o;
      }
    }
  } else {
#pragma unroll
    for (int j = 0; j < 2; ++j) {
#pragma unroll
      for (int q = 0; q < 4; ++q) {
        const int col = (2 * wn + j) * 32 + 8 * q + 4 * h;
        f32x4 cv = *(const f32x4*)(smem + P_CS + col * 4);
#pragma unroll
        for (int m = 0; m < MTW; ++m) {
          float v0 = (acc[m][j][4 * q] - rs[m].x * cv[0]) * rs[m].y, v1 = (acc[m][j][4 * q + 1] - rs[m].x * cv[1]) * rs[m].y;
          float v2 = (acc[m][j][4 * q + 2] - rs[m].x * cv[2]) * rs[m].y, v3 = (acc[m][j][4 * q + 3] - rs[m].x * cv[3]) * rs[m].y;
          u32x2 o; o[0] = pack2(v0, v1); o[1] = pack2(v2, v3);
          if (col < 240) *(u32x2*)(smem + P_QKV + ((wm * 3 + m) * 32 + r32) * QKV_STRIDE + col * 2) = o;
        }
      }
    }
    __syncthreads();
    STAMP(4);
    const float scale = 0.22360679774997896f;
    const int g = lane >> 4, c = lane & 15;
    const int q4 = c >> 2, p4 = c & 3;
    u32x2 res0[5], res1[5];
    bool okm[8]; int dlv[8];
#pragma unroll
    for (int i = 0; i < 8; ++i) {
      dlv[i] = 16 + c - (16 * (i >> 2) + 4 * g + (i & 3));
      okm[i] = dlv[i] >= 0 && dlv[i] <= 14 && !(dlv[i] & 1);
    }
    const f32x4 z4 = {0.f, 0.f, 0.f, 0.f};
    u32x4 qf[5], kf0[5], kf1[5];
    bf16x8 vf0[5], vf1[5];
    float lg[5][8], mx[5], sum[5];
#pragma unroll
    for (int it = 0; it < 5; ++it) {
      const int u0 = wave + 8 * it;
      const int u = u0 < 36 ? u0 : 35;
      const int rt = u >> 2, hl = u & 3;
      const char* base = smem + P_QKV + (16 * rt) * QKV_STRIDE + hl * 40;
      const char* qp = base + (16 + c) * QKV_STRIDE + 16 * g;
      u32x2 qa = *(const u32x2*)qp, qb = *(const u32x2*)(qp + 8);
      const char* kp = base + c * QKV_STRIDE + 160 + 16 * g;
      u32x2 k0a = *(const u32x2*)kp, k0b = *(const u32x2*)(kp + 8);
      u32x2 k1a = *(const u32x2*)(kp + 16 * QKV_STRIDE), k1b = *(const u32x2*)(kp + 16 * QKV_STRIDE + 8);
      const char* vp = base + (4 * g + q4) * QKV_STRIDE + 320 + 8 * p4;
      s16x4 v00 = tr_read(vp), v01 = tr_read(vp + 16 * QKV_STRIDE);
      s16x4 v10 = tr_read(vp + 32), v11 = tr_read(vp + 16 * QKV_STRIDE + 32);
      qf[it][0] = g < 3 ? qa[0] : 0u; qf[it][1] = g < 3 ? qa[1] : 0u; qf[it][2] = g < 2 ? qb[0] : 0u; qf[it][3] = g < 2 ? qb[1] : 0u;
      kf0[it][0] = k0a[0]; kf0[it][1] = k0a[1]; kf0[it][2] = k0b[0]; kf0[it][3] = k0b[1];
      kf1[it][0] = k1a[0]; kf1[it][1] = k1a[1]; kf1[it][2] = k1b[0]; kf1[it][3] = k1b[1];
      vf0[it] = __builtin_shufflevector(v00, v01, 0, 1, 2, 3, 4, 5, 6, 7);
      vf1[it] = __builtin_shufflevector(v10, v11, 0, 1, 2, 3, 4, 5, 6, 7);
    }
    SB(); STAMP(7); SB();
#pragma unroll
    for (int it = 0; it < 5; ++it) {
      const int u0 = wave + 8 * it;
      const int u = u0 < 36 ? u0 : 35;
      const int sq = rb * 144 + 16 * (u >> 2) + c;
      f32x4 s0 = __builtin_amdgcn_mfma_f32_16x16x32_bf16(__builtin_bit_cast(bf16x8, kf0[it]), __builtin_bit_cast(bf16x8, qf[it]), z4, 0, 0, 0);
      f32x4 s1 = __builtin_amdgcn_mfma_f32_16x16x32_bf16(__builtin_bit_cast(bf16x8, kf1[it]), __builtin_bit_cast(bf16x8, qf[it]), z4, 0, 0, 0);
#pragma unroll
      for (int i = 0; i < 8; ++i) {
        const float d = i < 4 ? s0[i & 3] : s1[i & 3];
        lg[it][i] = (okm[i] && dlv[i] <= sq) ? d * scale : -1e9f;
      }
      mx[it] = fmaxf(fmaxf(fmaxf(lg[it][0], lg[it][1]), fmaxf(lg[it][2], lg[it][3])),
                     fmaxf(fmaxf(lg[it][4], lg[it][5]), fmaxf(lg[it][6], lg[it][7])));
    }
    SB();
#pragma unroll
    for (int it = 0; it < 5; ++it) mx[it] = fmaxf(mx[it], __shfl_xor(mx[it], 16));
    SB();
#pragma unroll
    for (int it = 0; it < 5; ++it) mx[it] = fmaxf(mx[it], __shfl_xor(mx[it], 32));
    SB();
#pragma unroll
    for (int it = 0; it < 5; ++it) {
      float sm = 0.f;
#pragma unroll
      for (int i = 0; i < 8; ++i) { lg[it][i] = __expf(lg[it][i] - mx[it]); sm += lg[it][i]; }
      sum[it] = sm;
    }
    SB();
#pragma unroll
    for (int it = 0; it < 5; ++it) sum[it] += __shfl_xor(sum[it], 16);
    SB();
#pragma unroll
    for (int it = 0; it < 5; ++it) sum[it] += __shfl_xor(sum[it], 32);
    SB();
#pragma unroll
    for (int it = 0; it < 5; ++it) {
      const float inv = __builtin_amdgcn_rcpf(sum[it]);
      u32x4 pf;
#pragma unroll
      for (int i = 0; i < 4; ++i) pf[i] = pack2(lg[it][2 * i] * inv, lg[it][2 * i + 1] * inv);
      f32x4 o0 = __builtin_amdgcn_mfma_f32_16x16x32_bf16(vf0[it], __builtin_bit_cast(bf16x8, pf), z4, 0, 0, 0);
      f32x4 o1 = __builtin_amdgcn_mfma_f32_16x16x32_bf16(vf1[it], __builtin_bit_cast(bf16x8, pf), z4, 0, 0, 0);
      res0[it][0] = pack2(o0[0], o0[1]); res0[it][1] = pack2(o0[2], o0[3]);
      res1[it][0] = pack2(o1[0], o1[1]); res1[it][1] = pack2(o1[2], o1[3]);
    }
    SB(); STAMP(6); SB();
#pragma unroll
    for (int it = 0; it < 5; ++it) {
      const int u0 = wave + 8 * it;
      const int rt = u0 >> 2, hl = u0 & 3;
      const int sq = rb * 144 + 16 * rt + c;
      if (sq < S_ && u0 < 36) {
        char* obase = act + (size_t)(sq >> 4) * KS2 * 1024 + (sq & 15) * 16;
        {
          const int col = cb * 80 + hl * 20 + 4 * g;
          const int ks = col >> 5, kk = col & 31;
          st8(obase + ks * 1024 + (2 * (kk >> 4) + ((kk >> 2) & 1)) * 256 + ((kk >> 3) & 1) * 8, res0[it]);
        }
        if (g == 0) {
          const int col = cb * 80 + hl * 20 + 16;
          const int ks = col >> 5, kk = col & 31;
          st8(obase + ks * 1024 + (2 * (kk >> 4) + ((kk >> 2) & 1)) * 256 + ((kk >> 3) & 1) * 8, res1[it]);
        }
      }
    }
  }
  STAMP(5);
}

__global__ __launch_bounds__(512, 2) void k1_kernel(const u16* __restrict__ x, const u16* __restrict__ wqkv,
                                                    const u16* __restrict__ wo, const u16* __restrict__ w1,
                                                    const u16* __restrict__ w3, const u16* __restrict__ w2,
                                                    char* __restrict__ ws) {
  __shared__ __attribute__((aligned(16))) char smem[K1_LDS];
  const int b = blockIdx.x, xcd = b & 7, idx = b >> 3;
  const int wave = __builtin_amdgcn_readfirstlane(threadIdx.x >> 6);
  if (idx < 15) {
    if (wave < 4) k1_body<true, 3, 0>(smem, x, wqkv, wqkv, ws, idx, xcd, wave, wo, w2, 0);
    else k1_body<true, 2, 1>(smem, x, wqkv, wqkv, ws, idx, xcd, wave, wo, w2, 0);
  } else {
    const int t = xcd * 17 + (idx - 15);
    if (t >= 132) return;
    if (wave < 4) k1_body<false, 3, 0>(smem, x, w1, w3, ws, t % 11, t / 11, wave, wo, w2, t);
    else k1_body<false, 3, 1>(smem, x, w1, w3, ws, t % 11, t / 11, wave, wo, w2, t);
    { char* stampbuf = ws + (64u << 20); (void)stampbuf; STAMP(6); }
  }
}

__global__ __launch_bounds__(256, 1) void k2_kernel(const u16* __restrict__ x, const char* __restrict__ ws,
                                                    u16* __restrict__ out) {
  __shared__ __attribute__((aligned(16))) char red[4 * 20 * 1024];
  const int b = blockIdx.x, xcd = b & 7, idx = b >> 3;
  const int rb = 4 * xcd + (idx >> 3), nb = idx & 7;
  STAMP(0);
  const int lane = threadIdx.x & 63;
  const int wave = __builtin_amdgcn_readfirstlane(threadIdx.x >> 6);
  const char* ap = ws + ((size_t)(rb * 4) * KS2 + 17 * wave) * 1024 + lane * 16;
  const char* wp = ws + WF_OFF + ((size_t)(17 * wave) * 40 + nb * 5) * 1024 + lane * 16;
  constexpr int DEP = 6;
  bf16x8 af[DEP][4], wf[DEP][5];
  f32x4 acc[4][5];
#pragma unroll
  for (int i = 0; i < 4; ++i)
#pragma unroll
    for (int j = 0; j < 5; ++j) acc[i][j] = (f32x4){0.f, 0.f, 0.f, 0.f};
  auto ld = [&](int st, int buf) {
#pragma unroll
    for (int i = 0; i < 4; ++i) af[buf][i] = *(const bf16x8*)(ap + (size_t)i * KS2 * 1024 + st * 1024);
#pragma unroll
    for (int j = 0; j < 5; ++j) wf[buf][j] = *(const bf16x8*)(wp + (size_t)st * 40960 + j * 1024);
  };
#pragma unroll
  for (int st = 0; st < DEP - 1; ++st) ld(st, st);
#pragma unroll
  for (int st = 0; st < 17; ++st) {
    if (st + DEP - 1 < 17) ld(st + DEP - 1, (st + DEP - 1) % DEP);
    __builtin_amdgcn_sched_barrier(0);
#pragma unroll
    for (int i = 0; i < 4; ++i)
#pragma unroll
      for (int j = 0; j < 5; ++j)
        acc[i][j] = __builtin_amdgcn_mfma_f32_16x16x32_bf16(wf[st % DEP][j], af[st % DEP][i], acc[i][j], 0, 0, 0);
    __builtin_amdgcn_sched_barrier(0);
  }
  STAMP(2);
#pragma unroll
  for (int i = 0; i < 4; ++i)
#pragma unroll
    for (int j = 0; j < 5; ++j) *(f32x4*)(red + ((wave * 20 + i * 5 + j) * 64 + lane) * 16) = acc[i][j];
  __syncthreads();
  const int g = lane >> 4, r = lane & 15;
  const int row = rb * 64 + wave * 16 + r;
#pragma unroll
  for (int j = 0; j < 5; ++j) {
    f32x4 s = *(const f32x4*)(red + ((0 * 20 + wave * 5 + j) * 64 + lane) * 16);
#pragma unroll
    for (int w = 1; w < 4; ++w) s += *(const f32x4*)(red + ((w * 20 + wave * 5 + j) * 64 + lane) * 16);
    const int col = nb * 80 + j * 16 + g * 4;
    u32x2 xv = *(const u32x2*)(x + (size_t)row * D_ + col);
    u32x2 o;
    o[0] = pack2(bflo(xv[0]) + s[0], bfhi(xv[0]) + s[1]);
    o[1] = pack2(bflo(xv[1]) + s[2], bfhi(xv[1]) + s[3]);
    *(u32x2*)(out + (size_t)row * D_ + col) = o;
  }
  STAMP(3);
}

}

extern "C" void kernel_launch(void* const* d_in, const int* in_sizes, int n_in, void* d_out, int out_size,
                              void* d_ws, size_t ws_size, hipStream_t stream) {
  const u16* x = (const u16*)d_in[0];
  const u16* wqkv = (const u16*)d_in[1];
  const u16* wo = (const u16*)d_in[2];
  const u16* w1 = (const u16*)d_in[4];
  const u16* w3 = (const u16*)d_in[5];
  const u16* w2 = (const u16*)d_in[6];
  k1_kernel<<<256, 512, 0, stream>>>(x, wqkv, wo, w1, w3, w2, (char*)d_ws);
  k2_kernel<<<256, 256, 0, stream>>>(x, (const char*)d_ws, (u16*)d_out);
}
```

```cpp
#include <hip/hip_runtime.h>
#include <hip/hip_bf16.h>
#include <stdint.h>
#include <type_traits>

typedef __attribute__((ext_vector_type(8))) short bf16x8;
typedef __attribute__((ext_vector_type(4))) short s16x4;
typedef __attribute__((ext_vector_type(2))) __bf16 bf16x2v;
typedef __attribute__((ext_vector_type(4))) float f32x4;
typedef __attribute__((ext_vector_type(16))) float f32x16;
typedef __attribute__((ext_vector_type(4))) unsigned u32x4;
typedef __attribute__((ext_vector_type(2))) unsigned u32x2;
typedef unsigned short u16;
#define LDS_AS __attribute__((address_space(3)))

#define STAMP(i) do { } while (0)

namespace {

constexpr int S_ = 2048, D_ = 640, F_ = 1536;
constexpr int KS2 = 68;
constexpr size_t ACT_BYTES = (size_t)128 * KS2 * 1024;
constexpr size_t WF_OFF = ACT_BYTES;

constexpr int XSLOT = 24576, NXS = 3;
constexpr int WSLOT = 16384, NWS = 5;
constexpr int WRING = XSLOT * NXS;
constexpr int ONES_OFF = WRING + WSLOT * NWS;
constexpr int K1_LDS = ONES_OFF + 64;
constexpr int P_STAT = 0;
constexpr int P_CS = 12288;
constexpr int P_ROW = 13312;
constexpr int P_QKV = 16384;
constexpr int QKV_STRIDE = 488;

#define WAIT_V(n) asm volatile("s_waitcnt vmcnt(%0)" ::"n"(n) : "memory")
#define WAIT_L0() asm volatile("s_waitcnt lgkmcnt(0)" ::: "memory")
#define RAW_BARRIER() do { WAIT_L0(); __builtin_amdgcn_s_barrier(); } while (0)

__device__ __forceinline__ float bfr(float f) { return (float)(__bf16)f; }
__device__ __forceinline__ unsigned pack2(float a, float b) {
  bf16x2v v; v[0] = (__bf16)a; v[1] = (__bf16)b; return __builtin_bit_cast(unsigned, v);
}
__device__ __forceinline__ float bflo(unsigned u) { return __builtin_bit_cast(float, u << 16); }
__device__ __forceinline__ float bfhi(unsigned u) { return __builtin_bit_cast(float, u & 0xffff0000u); }
__device__ __forceinline__ float dot2(unsigned a, unsigned b, float c) {
  return __builtin_amdgcn_fdot2_f32_bf16(__builtin_bit_cast(bf16x2v, a), __builtin_bit_cast(bf16x2v, b), c, false);
}
__device__ __forceinline__ void vadd(float& a, float b) { asm("v_add_f32 %0, %0, %1" : "+v"(a) : "v"(b)); }
__device__ __forceinline__ void vsq(float& a, float b) { asm("v_fmac_f32 %0, %1, %1" : "+v"(a) : "v"(b)); }
__device__ __forceinline__ void st16(void* p, u32x4 v) {
  asm volatile("global_store_dwordx4 %0, %1, off sc1\n\ts_nop 1" ::"v"(p), "v"(v) : "memory");
}
__device__ __forceinline__ void st8(void* p, u32x2 v) {
  asm volatile("global_store_dwordx2 %0, %1, off sc1\n\ts_nop 1" ::"v"(p), "v"(v) : "memory");
}
__device__ __forceinline__ void dma16(const void* g, char* lds_uniform) {
  __builtin_amdgcn_global_load_lds((const unsigned*)g, (unsigned*)lds_uniform, 16, 0, 0);
}
__device__ __forceinline__ s16x4 tr_read(const char* p) {
  return __builtin_amdgcn_ds_read_tr16_b64_v4i16((LDS_AS s16x4*)p);
}

#define SB() __builtin_amdgcn_sched_barrier(0)

__device__ __forceinline__ void attention_phase(const char* smem, char* __restrict__ act, int rb, int cb, int wave,
                                                int lane) {
  constexpr int NU = 3;
  const float scale = 0.22360679774997896f * 1.4426950408889634f;
  const int g = lane >> 4, c = lane & 15;
  const int q4 = c >> 2, p4 = c & 3;
  bool okm[8]; int dlv[8];
#pragma unroll
  for (int i = 0; i < 8; ++i) {
    dlv[i] = 16 + c - (16 * (i >> 2) + 4 * g + (i & 3));
    okm[i] = dlv[i] >= 0 && dlv[i] <= 14 && !(dlv[i] & 1);
  }
  const f32x4 z4 = {0.f, 0.f, 0.f, 0.f};
  u32x4 qf[NU], kf0[NU], kf1[NU];
  bf16x8 vf0[NU], vf1[NU];
  float lg[NU][8], mx[NU], sum[NU];
#pragma unroll
  for (int it = 0; it < NU; ++it) {
    const int u = wave + 12 * it;
    const int rt = u >> 2, hl = u & 3;
    const char* base = smem + P_QKV + (16 * rt) * QKV_STRIDE + hl * 40;
    const char* qp = base + (16 + c) * QKV_STRIDE + 16 * g;
    u32x2 qa = *(const u32x2*)qp, qb = *(const u32x2*)(qp + 8);
    const char* kp = base + c * QKV_STRIDE + 160 + 16 * g;
    u32x2 k0a = *(const u32x2*)kp, k0b = *(const u32x2*)(kp + 8);
    u32x2 k1a = *(const u32x2*)(kp + 16 * QKV_STRIDE), k1b = *(const u32x2*)(kp + 16 * QKV_STRIDE + 8);
    const char* vp = base + (4 * g + q4) * QKV_STRIDE + 320 + 8 * p4;
    s16x4 v00 = tr_read(vp), v01 = tr_read(vp + 16 * QKV_STRIDE);
    s16x4 v10 = tr_read(vp + 32), v11 = tr_read(vp + 16 * QKV_STRIDE + 32);
    qf[it][0] = g < 3 ? qa[0] : 0u; qf[it][1] = g < 3 ? qa[1] : 0u; qf[it][2] = g < 2 ? qb[0] : 0u; qf[it][3] = g < 2 ? qb[1] : 0u;
    kf0[it][0] = k0a[0]; kf0[it][1] = k0a[1]; kf0[it][2] = k0b[0]; kf0[it][3] = k0b[1];
    kf1[it][0] = k1a[0]; kf1[it][1] = k1a[1]; kf1[it][2] = k1b[0]; kf1[it][3] = k1b[1];
    vf0[it] = __builtin_shufflevector(v00, v01, 0, 1, 2, 3, 4, 5, 6, 7);
    vf1[it] = __builtin_shufflevector(v10, v11, 0, 1, 2, 3, 4, 5, 6, 7);
  }
  SB();
#pragma unroll
  for (int it = 0; it < NU; ++it) {
    const int u = wave + 12 * it;
    const int sq = rb * 144 + 16 * (u >> 2) + c;
    f32x4 s0 = __builtin_amdgcn_mfma_f32_16x16x32_bf16(__builtin_bit_cast(bf16x8, kf0[it]), __builtin_bit_cast(bf16x8, qf[it]), z4, 0, 0, 0);
    f32x4 s1 = __builtin_amdgcn_mfma_f32_16x16x32_bf16(__builtin_bit_cast(bf16x8, kf1[it]), __builtin_bit_cast(bf16x8, qf[it]), z4, 0, 0, 0);
#pragma unroll
    for (int i = 0; i < 8; ++i) {
      const float d = i < 4 ? s0[i & 3] : s1[i & 3];
      lg[it][i] = (okm[i] && dlv[i] <= sq) ? d * scale : -1e9f;
    }
    mx[it] = fmaxf(fmaxf(fmaxf(lg[it][0], lg[it][1]), fmaxf(lg[it][2], lg[it][3])),
                   fmaxf(fmaxf(lg[it][4], lg[it][5]), fmaxf(lg[it][6], lg[it][7])));
  }
  SB();
#pragma unroll
  for (int it = 0; it < NU; ++it) mx[it] = fmaxf(mx[it], __shfl_xor(mx[it], 16));
  SB();
#pragma unroll
  for (int it = 0; it < NU; ++it) mx[it] = fmaxf(mx[it], __shfl_xor(mx[it], 32));
  SB();
#pragma unroll
  for (int it = 0; it < NU; ++it) {
    float sm = 0.f;
#pragma unroll
    for (int i = 0; i < 8; ++i) { lg[it][i] = __builtin_amdgcn_exp2f(lg[it][i] - mx[it]); sm += lg[it][i]; }
    sum[it] = sm;
  }
  SB();
#pragma unroll
  for (int it = 0; it < NU; ++it) sum[it] += __shfl_xor(sum[it], 16);
  SB();
#pragma unroll
  for (int it = 0; it < NU; ++it) sum[it] += __shfl_xor(sum[it], 32);
  SB();
  u32x2 res0[NU], res1[NU];
#pragma unroll
  for (int it = 0; it < NU; ++it) {
    const float inv = __builtin_amdgcn_rcpf(sum[it]);
    u32x4 pf;
#pragma unroll
    for (int i = 0; i < 4; ++i) pf[i] = pack2(lg[it][2 * i] * inv, lg[it][2 * i + 1] * inv);
    f32x4 o0 = __builtin_amdgcn_mfma_f32_16x16x32_bf16(vf0[it], __builtin_bit_cast(bf16x8, pf), z4, 0, 0, 0);
    f32x4 o1 = __builtin_amdgcn_mfma_f32_16x16x32_bf16(vf1[it], __builtin_bit_cast(bf16x8, pf), z4, 0, 0, 0);
    res0[it][0] = pack2(o0[0], o0[1]); res0[it][1] = pack2(o0[2], o0[3]);
    res1[it][0] = pack2(o1[0], o1[1]); res1[it][1] = pack2(o1[2], o1[3]);
  }
#pragma unroll
  for (int it = 0; it < NU; ++it) {
    const int u = wave + 12 * it;
    const int rt = u >> 2, hl = u & 3;
    const int sq = rb * 144 + 16 * rt + c;
    if (sq < S_) {
      char* obase = act + (size_t)(sq >> 4) * KS2 * 1024 + (sq & 15) * 16;
      {
        const int col = cb * 80 + hl * 20 + 4 * g;
        const int ks = col >> 5, kk = col & 31;
        st8(obase + ks * 1024 + (2 * (kk >> 4) + ((kk >> 2) & 1)) * 256 + ((kk >> 3) & 1) * 8, res0[it]);
      }
      if (g == 0) {
        const int col = cb * 80 + hl * 20 + 16;
        const int ks = col >> 5, kk = col & 31;
        st8(obase + ks * 1024 + (2 * (kk >> 4) + ((kk >> 2) & 1)) * 256 + ((kk >> 3) & 1) * 8, res1[it]);
      }
    }
  }
}

template <bool QKV, int MTW>
__device__ __forceinline__ void k1_compute(char* smem, const u16* __restrict__ wa, const u16* __restrict__ wb,
                                           char* __restrict__ act, int rb, int cb, const int wave) {
  char* stampbuf = act + (64u << 20); (void)stampbuf;
  STAMP(0);
  const int tid = threadIdx.x, lane = tid & 63;
  const int wm = wave >> 2, wn = wave & 3;
  const int h = lane >> 5, r32 = lane & 31;
  const int row0 = QKV ? rb * 144 - 16 : rb * 191 - 1;

  const char* wsrc[2];
#pragma unroll
  for (int j = 0; j < 2; ++j) {
    int k = 4 * wave + 2 * j + (lane >> 5);
    int c = (lane & 31) ^ ((k & 3) << 2);
    if (QKV) {
      int cc = c > 29 ? 29 : c;
      int seg = cc / 10, cq = cc - seg * 10;
      wsrc[j] = (const char*)wa + ((size_t)k * 1920 + seg * 640 + cb * 80 + cq * 8) * 2;
    } else {
      int wv = c >> 2, part = (c >> 1) & 1, hf = c & 1;
      wsrc[j] = (const char*)(part ? wb : wa) + ((size_t)k * F_ + cb * 128 + wv * 16 + hf * 8) * 2;
    }
  }
  constexpr int WADV2 = (QKV ? 1920 : F_) * 2 * 64;
  const char* wsrc0 = wsrc[0];
  const int wdelta = (int)(wsrc[1] - wsrc[0]);
  auto dmaW = [&](int j, int slot) {
    dma16(j ? wsrc0 + wdelta : wsrc0, smem + WRING + slot * WSLOT + wave * 2048 + j * 1024);
    if (j) wsrc0 += WADV2;
  };

  const int xoff0 = (wm * 96 + r32) * 128 + ((h ^ ((r32 >> 1) & 7)) * 16);
  const bool onesl = (wm == 0) && (r32 == 0);
  int woff0;
  {
    int blk = (lane >> 4) & 1, q4 = (lane & 15) >> 2, p = lane & 3;
    woff0 = WRING + (8 * h + q4) * 512 + (((8 * wn + 2 * blk + (p >> 1)) ^ (q4 << 2)) * 16) + (p & 1) * 8;
  }

  f32x16 acc[MTW][2];
#pragma unroll
  for (int m = 0; m < MTW; ++m)
#pragma unroll
    for (int i = 0; i < 16; ++i) { acc[m][0][i] = 0.f; acc[m][1][i] = 0.f; }

  bf16x8 xf0[MTW], xf1[MTW], wf0[2], wf1[2];
  auto LX = [&](bf16x8(&xf)[MTW], int m, int xbase, int q) {
    if (m >= MTW) return;
    const int a = xbase + (xoff0 ^ (q * 32));
    if (m == 0) xf[0] = *(const bf16x8*)(smem + (onesl ? ONES_OFF : a));
    else xf[m] = *(const bf16x8*)(smem + a + m * 4096);
  };
  auto LW = [&](bf16x8(&wf)[2], int j, int wbase, int k16) {
    const int o = wbase + (j ? (woff0 ^ 64) : woff0) + k16 * 8192;
    s16x4 a0 = tr_read(smem + o);
    s16x4 a1 = tr_read(smem + o + 2048);
    wf[j] = __builtin_shufflevector(a0, a1, 0, 1, 2, 3, 4, 5, 6, 7);
  };
  auto MF = [&](int i, bf16x8(&xf)[MTW], bf16x8(&wf)[2]) {
    SB();
    if ((i >> 1) < MTW)
      acc[i >> 1][i & 1] = __builtin_amdgcn_mfma_f32_32x32x16_bf16(wf[i & 1], xf[i >> 1], acc[i >> 1][i & 1], 0, 0, 0);
    SB();
  };
  auto sub = [&](bf16x8(&xc)[MTW], bf16x8(&wc)[2], bf16x8(&xn)[MTW], bf16x8(&wn2)[2], int xbn, int qn, int wbn,
                 int k16n, int dslot) {
    MF(0, xc, wc); LW(wn2, 0, wbn, k16n);
    MF(1, xc, wc); LW(wn2, 1, wbn, k16n); LX(xn, 0, xbn, qn);
    MF(2, xc, wc); LX(xn, 1, xbn, qn); LX(xn, 2, xbn, qn);
    MF(3, xc, wc); if (dslot >= 0) dmaW(0, dslot);
    MF(4, xc, wc); if (dslot >= 0) dmaW(1, dslot);
    MF(5, xc, wc);
    SB();
  };

  if (tid == 0) *(u32x4*)(smem + ONES_OFF) = (u32x4){0x3f803f80u, 0x3f803f80u, 0x3f803f80u, 0x3f803f80u};
  dmaW(0, 0); dmaW(1, 0); dmaW(0, 2); dmaW(1, 2);
  WAIT_V(2);
  RAW_BARRIER();
  STAMP(1);
  LX(xf0, 0, 0, 0); LX(xf0, 1, 0, 0); LX(xf0, 2, 0, 0); LW(wf0, 0, 0, 0); LW(wf0, 1, 0, 0);

  int ws0 = 0;
  int xs0 = 0;
  auto pair_steps = [&](auto tailc) {
    constexpr int TAIL = decltype(tailc)::value;
    {
      RAW_BARRIER();
      int ws1 = ws0 + 1; if (ws1 >= NWS) ws1 -= NWS;
      int ws4 = ws0 + 4; if (ws4 >= NWS) ws4 -= NWS;
      const int xb = xs0 * XSLOT, wb0 = ws0 * WSLOT, wb1 = ws1 * WSLOT;
      sub(xf0, wf0, xf1, wf1, xb, 1, wb0, 1, TAIL ? -1 : ws4);
      sub(xf1, wf1, xf0, wf0, xb, 2, wb1, 0, -1);
      ws0 = ws1;
    }
    {
      if (TAIL == 0) WAIT_V(2);
      if (TAIL == 1) WAIT_V(0);
      RAW_BARRIER();
      int ws1 = ws0 + 1; if (ws1 >= NWS) ws1 -= NWS;
      int xs1 = xs0 + 1; if (xs1 >= NXS) xs1 -= NXS;
      const int xb = xs0 * XSLOT, xb1 = xs1 * XSLOT, wb0 = ws0 * WSLOT, wb1 = ws1 * WSLOT;
      sub(xf0, wf0, xf1, wf1, xb, 3, wb0, 1, -1);
      sub(xf1, wf1, xf0, wf0, xb1, 0, wb1, 0, -1);
      ws0 = ws1; xs0 = xs1;
    }
  };
  for (int s = 0; s < 16; s += 2) pair_steps(std::integral_constant<int, 0>{});
  pair_steps(std::integral_constant<int, 1>{});
  pair_steps(std::integral_constant<int, 2>{});

  RAW_BARRIER();
  STAMP(2);
  if (onesl) {
#pragma unroll
    for (int j = 0; j < 2; ++j)
#pragma unroll
      for (int q = 0; q < 4; ++q)
        *(f32x4*)(smem + P_CS + ((2 * wn + j) * 32 + 8 * q + 4 * h) * 4) =
            (f32x4){acc[0][j][4 * q], acc[0][j][4 * q + 1], acc[0][j][4 * q + 2], acc[0][j][4 * q + 3]};
  }
  RAW_BARRIER();
  STAMP(3);

  float2 rs[MTW];
#pragma unroll
  for (int m = 0; m < MTW; ++m) rs[m] = ((const float2*)(smem + P_ROW))[(wm * 3 + m) * 32 + r32];

  if (!QKV) {
    const int ks = 20 + cb * 4 + wn;
#pragma unroll
    for (int j = 0; j < 2; ++j) {
      float csv[16];
#pragma unroll
      for (int q = 0; q < 4; ++q) {
        f32x4 v = *(const f32x4*)(smem + P_CS + ((2 * wn + j) * 32 + 8 * q + 4 * h) * 4);
        csv[4 * q] = v[0]; csv[4 * q + 1] = v[1]; csv[4 * q + 2] = v[2]; csv[4 * q + 3] = v[3];
      }
#pragma unroll
      for (int m = 0; m < MTW; ++m) {
        float hv[8];
#pragma unroll
        for (int i = 0; i < 8; ++i) {
          float g = (acc[m][j][i] - rs[m].x * csv[i]) * rs[m].y;
          float u = (acc[m][j][8 + i] - rs[m].x * csv[8 + i]) * rs[m].y;
          hv[i] = g * __builtin_amdgcn_rcpf(1.f + __expf(-g)) * u;
        }
        int grow = row0 + (wm * 3 + m) * 32 + r32;
        if (grow < S_ && !(m == 0 && onesl)) {
          u32x4 o; o[0] = pack2(hv[0], hv[1]); o[1] = pack2(hv[2], hv[3]); o[2] = pack2(hv[4], hv[5]); o[3] = pack2(hv[6], hv[7]);
          st16(act + ((size_t)(grow >> 4) * KS2 + ks) * 1024 + ((2 * j + h) * 16 + (grow & 15)) * 16, o);
        }
      }
    }
  } else {
#pragma unroll
    for (int j = 0; j < 2; ++j) {
#pragma unroll
      for (int q = 0; q < 4; ++q) {
        const int col = (2 * wn + j) * 32 + 8 * q + 4 * h;
        f32x4 cv = *(const f32x4*)(smem + P_CS + col * 4);
#pragma unroll
        for (int m = 0; m < MTW; ++m) {
          float v0 = (acc[m][j][4 * q] - rs[m].x * cv[0]) * rs[m].y, v1 = (acc[m][j][4 * q + 1] - rs[m].x * cv[1]) * rs[m].y;
          float v2 = (acc[m][j][4 * q + 2] - rs[m].x * cv[2]) * rs[m].y, v3 = (acc[m][j][4 * q + 3] - rs[m].x * cv[3]) * rs[m].y;
          u32x2 o; o[0] = pack2(v0, v1); o[1] = pack2(v2, v3);
          if (col < 240) *(u32x2*)(smem + P_QKV + ((wm * 3 + m) * 32 + r32) * QKV_STRIDE + col * 2) = o;
        }
      }
    }
    RAW_BARRIER();
    STAMP(4);
    attention_phase(smem, act, rb, cb, wave, lane);
  }
  STAMP(5);
}

template <bool QKV>
__device__ __forceinline__ void k1_helper(char* smem, const u16* __restrict__ x, const u16* __restrict__ wa,
                                          const u16* __restrict__ wb, char* __restrict__ act, int rb, int cb,
                                          const int wave, const u16* __restrict__ wo, const u16* __restrict__ w2,
                                          int t) {
  constexpr int BMR = QKV ? 160 : 192;
  constexpr int NX = BMR / 32;
  const int lane = threadIdx.x & 63;
  const int hw = wave - 8;
  const int b2 = blockIdx.x & 7; (void)b2;
  const int row0 = QKV ? rb * 144 - 16 : rb * 191 - 1;

  const int hid = hw * 64 + lane;
  const int item = t * 165 + hid;
  const bool doit = !QKV && hid < 165 && item < KS2 * 4 * 80;
  u32x4 r[8];
  if (!QKV) {
    const int it2 = doit ? item : 0;
    const int c8 = it2 % 80, kg = it2 / 80, g = kg & 3, ks = kg >> 2;
    const int k0 = 32 * ks + 16 * (g >> 1) + 4 * (g & 1);
    const u16* src = (ks < 20 ? wo + (size_t)k0 * D_ : w2 + (size_t)(k0 - D_) * D_) + c8 * 8;
#pragma unroll
    for (int j = 0; j < 8; ++j) r[j] = *(const u32x4*)(src + (size_t)(8 * (j >> 2) + (j & 3)) * D_);
  }

  const char* xsrc[NX];
#pragma unroll
  for (int j = 0; j < NX; ++j) {
    int lr = 8 * (hw * NX + j) + (lane >> 3);
    int c = (lane & 7) ^ ((lr >> 1) & 7);
    int gr = row0 + lr; gr = gr < 0 ? 0 : (gr > S_ - 1 ? S_ - 1 : gr);
    xsrc[j] = (const char*)x + (size_t)gr * (D_ * 2) + c * 16;
  }
  const char* wsrc[4];
#pragma unroll
  for (int j = 0; j < 4; ++j) {
    int k = 8 * hw + 2 * j + (lane >> 5);
    int c = (lane & 31) ^ ((k & 3) << 2);
    if (QKV) {
      int cc = c > 29 ? 29 : c;
      int seg = cc / 10, cq = cc - seg * 10;
      wsrc[j] = (const char*)wa + ((size_t)k * 1920 + seg * 640 + cb * 80 + cq * 8) * 2;
    } else {
      int wv = c >> 2, part = (c >> 1) & 1, hf = c & 1;
      wsrc[j] = (const char*)(part ? wb : wa) + ((size_t)k * F_ + cb * 128 + wv * 16 + hf * 8) * 2;
    }
  }
  constexpr int WADV = (QKV ? 1920 : F_) * 2 * 64;
#pragma unroll
  for (int j = 0; j < 4; ++j) wsrc[j] += WADV / 2;
  auto issueW = [&](int slot) {
#pragma unroll
    for (int j = 0; j < 4; ++j) { dma16(wsrc[j], smem + WRING + slot * WSLOT + hw * 4096 + j * 1024); wsrc[j] += WADV; }
  };
  auto issueX = [&](int slot) {
#pragma unroll
    for (int j = 0; j < NX; ++j) { dma16(xsrc[j], smem + slot * XSLOT + (hw * NX + j) * 1024); xsrc[j] += 128; }
  };

  float sx[NX][2], sxx[NX][2];
#pragma unroll
  for (int i = 0; i < NX; ++i) { sx[i][0] = sx[i][1] = 0.f; sxx[i][0] = sxx[i][1] = 0.f; }
  const int soff = hw * NX * 1024 + lane * 16;
  u32x4 sv[NX];
  auto stats_ld = [&](int xbase) {
#pragma unroll
    for (int i = 0; i < NX; ++i) sv[i] = *(const u32x4*)(smem + xbase + soff + i * 1024);
  };
  auto stats_acc = [&](int k) {
    if (k >= 2 * NX) return;
    const int i = k >> 1;
#pragma unroll
    for (int d = (k & 1) * 2; d < (k & 1) * 2 + 2; ++d) {
      float lo = bflo(sv[i][d]), hi = bfhi(sv[i][d]);
      vadd(sx[i][0], lo); vadd(sx[i][1], hi);
      vsq(sxx[i][0], lo); vsq(sxx[i][1], hi);
    }
  };
  auto stats_all = [&](int xbase) {
    stats_ld(xbase);
#pragma unroll
    for (int k = 0; k < 2 * NX; ++k) stats_acc(k);
  };

  issueX(0); issueW(1); issueX(1); issueW(3);
  WAIT_V(8 + NX);
  RAW_BARRIER();
  int ws5 = 0, xs0 = 0;
  for (int s = 0; s < 16; s += 2) {
    WAIT_V(4 + NX); RAW_BARRIER();
    {
      int xs2 = xs0 + 2; if (xs2 >= NXS) xs2 -= NXS;
      stats_ld(xs0 * XSLOT);
#pragma unroll
      for (int j = 0; j < NX; ++j) {
        SB(); dma16(xsrc[j], smem + xs2 * XSLOT + (hw * NX + j) * 1024); xsrc[j] += 128; SB();
        if (j) stats_acc(j - 1);
      }
      stats_acc(NX - 1);
    }
    WAIT_V(4 + NX); RAW_BARRIER();
#pragma unroll
    for (int j = 0; j < 4; ++j) {
      SB(); dma16(wsrc[j], smem + WRING + ws5 * WSLOT + hw * 4096 + j * 1024); wsrc[j] += WADV; SB();
      stats_acc(NX + j);
    }
    stats_acc(NX + 4); stats_acc(NX + 5);
    ws5 += 2; if (ws5 >= NWS) ws5 -= NWS;
    if (++xs0 >= NXS) xs0 = 0;
  }
  WAIT_V(4 + NX); RAW_BARRIER();
  stats_all(xs0 * XSLOT);
  if (++xs0 >= NXS) xs0 = 0;
  WAIT_V(4); RAW_BARRIER();
  WAIT_V(0); RAW_BARRIER();
  stats_all(xs0 * XSLOT);
  RAW_BARRIER();
  float2 rsv[NX];
#pragma unroll
  for (int i = 0; i < NX; ++i) {
    float a = sx[i][0] + sx[i][1], b = sxx[i][0] + sxx[i][1];
    a += __shfl_xor(a, 1); b += __shfl_xor(b, 1);
    a += __shfl_xor(a, 2); b += __shfl_xor(b, 2);
    a += __shfl_xor(a, 4); b += __shfl_xor(b, 4);
    float mean = a * (1.f / D_);
    float var = b * (1.f / D_) - mean * mean; var = var < 0.f ? 0.f : var;
    float mb = bfr(mean);
    float den = bfr(sqrtf(bfr(bfr(var) + 1e-5f)));
    rsv[i] = make_float2(mb, 1.f / den);
  }
  RAW_BARRIER();
#pragma unroll
  for (int i = 0; i < NX; ++i)
    if ((lane & 7) == 0) ((float2*)(smem + P_ROW))[8 * (hw * NX + i) + (lane >> 3)] = rsv[i];
  RAW_BARRIER();
  if (!QKV) {
    if (doit) {
      const int c8 = item % 80, kg = item / 80, g = kg & 3, ks2 = kg >> 2;
      char* dst = act + WF_OFF + ((size_t)(ks2 * 40 + (c8 >> 1)) * 64 + g * 16 + (c8 & 1) * 8) * 16;
#pragma unroll
      for (int c = 0; c < 8; ++c) {
        u32x4 o;
#pragma unroll
        for (int d = 0; d < 4; ++d)
          o[d] = __builtin_amdgcn_perm(r[2 * d + 1][c >> 1], r[2 * d][c >> 1], (c & 1) ? 0x07060302u : 0x05040100u);
        *(u32x4*)(dst + c * 16) = o;
      }
    }
  } else {
    RAW_BARRIER();
    attention_phase(smem, act, rb, cb, wave, lane);
  }
}

__global__ __launch_bounds__(768, 3) void k1_kernel(const u16* __restrict__ x, const u16* __restrict__ wqkv,
                                                    const u16* __restrict__ wo, const u16* __restrict__ w1,
                                                    const u16* __restrict__ w3, const u16* __restrict__ w2,
                                                    char* __restrict__ ws) {
  __shared__ __attribute__((aligned(16))) char smem[K1_LDS];
  const int b = blockIdx.x, xcd = b & 7, idx = b >> 3;
  const int wave = __builtin_amdgcn_readfirstlane(threadIdx.x >> 6);
  if (idx < 15) {
    if (wave < 4) k1_compute<true, 3>(smem, wqkv, wqkv, ws, idx, xcd, wave);
    else if (wave < 8) k1_compute<true, 2>(smem, wqkv, wqkv, ws, idx, xcd, wave);
    else k1_helper<true>(smem, x, wqkv, wqkv, ws, idx, xcd, wave, wo, w2, 0);
  } else {
    const int t = xcd * 17 + (idx - 15);
    if (t >= 132) return;
    if (wave < 8) k1_compute<false, 3>(smem, w1, w3, ws, t % 11, t / 11, wave);
    else k1_helper<false>(smem, x, w1, w3, ws, t % 11, t / 11, wave, wo, w2, t);
  }
}

__global__ __launch_bounds__(256, 1) void k2_kernel(const u16* __restrict__ x, const char* __restrict__ ws,
                                                    u16* __restrict__ out) {
  __shared__ __attribute__((aligned(16))) char red[4 * 20 * 1024];
  const int b = blockIdx.x, xcd = b & 7, idx = b >> 3;
  const int rb = 4 * xcd + (idx >> 3), nb = idx & 7;
  STAMP(0);
  const int lane = threadIdx.x & 63;
  const int wave = __builtin_amdgcn_readfirstlane(threadIdx.x >> 6);
  const char* ap = ws + ((size_t)(rb * 4) * KS2 + 17 * wave) * 1024 + lane * 16;
  const char* wp = ws + WF_OFF + ((size_t)(17 * wave) * 40 + nb * 5) * 1024 + lane * 16;
  constexpr int DEP = 6;
  bf16x8 af[DEP][4], wf[DEP][5];
  f32x4 acc[4][5];
#pragma unroll
  for (int i = 0; i < 4; ++i)
#pragma unroll
    for (int j = 0; j < 5; ++j) acc[i][j] = (f32x4){0.f, 0.f, 0.f, 0.f};
  auto ld = [&](int st, int buf) {
#pragma unroll
    for (int i = 0; i < 4; ++i) af[buf][i] = *(const bf16x8*)(ap + (size_t)i * KS2 * 1024 + st * 1024);
#pragma unroll
    for (int j = 0; j < 5; ++j) wf[buf][j] = *(const bf16x8*)(wp + (size_t)st * 40960 + j * 1024);
  };
#pragma unroll
  for (int st = 0; st < DEP - 1; ++st) ld(st, st);
#pragma unroll
  for (int st = 0; st < 17; ++st) {
    if (st + DEP - 1 < 17) ld(st + DEP - 1, (st + DEP - 1) % DEP);
    __builtin_amdgcn_sched_barrier(0);
#pragma unroll
    for (int i = 0; i < 4; ++i)
#pragma unroll
      for (int j = 0; j < 5; ++j)
        acc[i][j] = __builtin_amdgcn_mfma_f32_16x16x32_bf16(wf[st % DEP][j], af[st % DEP][i], acc[i][j], 0, 0, 0);
    __builtin_amdgcn_sched_barrier(0);
  }
  STAMP(2);
#pragma unroll
  for (int i = 0; i < 4; ++i)
#pragma unroll
    for (int j = 0; j < 5; ++j) *(f32x4*)(red + ((wave * 20 + i * 5 + j) * 64 + lane) * 16) = acc[i][j];
  __syncthreads();
  const int g = lane >> 4, r = lane & 15;
  const int row = rb * 64 + wave * 16 + r;
#pragma unroll
  for (int j = 0; j < 5; ++j) {
    f32x4 s = *(const f32x4*)(red + ((0 * 20 + wave * 5 + j) * 64 + lane) * 16);
#pragma unroll
    for (int w = 1; w < 4; ++w) s += *(const f32x4*)(red + ((w * 20 + wave * 5 + j) * 64 + lane) * 16);
    const int col = nb * 80 + j * 16 + g * 4;
    u32x2 xv = *(const u32x2*)(x + (size_t)row * D_ + col);
    u32x2 o;
    o[0] = pack2(bflo(xv[0]) + s[0], bfhi(xv[0]) + s[1]);
    o[1] = pack2(bflo(xv[1]) + s[2], bfhi(xv[1]) + s[3]);
    *(u32x2*)(out + (size_t)row * D_ + col) = o;
  }
  STAMP(3);
}

}

extern "C" void kernel_launch(void* const* d_in, const int* in_sizes, int n_in, void* d_out, int out_size,
                              void* d_ws, size_t ws_size, hipStream_t stream) {
  const u16* x = (const u16*)d_in[0];
  const u16* wqkv = (const u16*)d_in[1];
  const u16* wo = (const u16*)d_in[2];
  const u16* w1 = (const u16*)d_in[4];
  const u16* w3 = (const u16*)d_in[5];
  const u16* w2 = (const u16*)d_in[6];
  k1_kernel<<<256, 768, 0, stream>>>(x, wqkv, wo, w1, w3, w2, (char*)d_ws);
  k2_kernel<<<256, 256, 0, stream>>>(x, (const char*)d_ws, (u16*)d_out);
}
```

```cpp
#include <hip/hip_runtime.h>
#include <hip/hip_bf16.h>
#include <stdint.h>
#include <type_traits>

typedef __attribute__((ext_vector_type(8))) short bf16x8;
typedef __attribute__((ext_vector_type(4))) short s16x4;
typedef __attribute__((ext_vector_type(2))) __bf16 bf16x2v;
typedef __attribute__((ext_vector_type(4))) float f32x4;
typedef __attribute__((ext_vector_type(16))) float f32x16;
typedef __attribute__((ext_vector_type(4))) unsigned u32x4;
typedef __attribute__((ext_vector_type(2))) unsigned u32x2;
typedef unsigned short u16;
#define LDS_AS __attribute__((address_space(3)))

#define STAMP(i) do { } while (0)

namespace {

constexpr int S_ = 2048, D_ = 640, F_ = 1536;
constexpr int KS2 = 68;
constexpr size_t ACT_BYTES = (size_t)128 * KS2 * 1024;
constexpr size_t WF_OFF = ACT_BYTES;

constexpr size_t MB_OFF = WF_OFF + (size_t)KS2 * 40 * 1024;
constexpr unsigned long long MAGIC = 0x5A17C3E98B2D64F1ull;

constexpr int XSLOT = 24576, NXS = 3;
constexpr int WSLOT = 16384, NWS = 5;
constexpr int WRING = XSLOT * NXS;
constexpr int ONES_OFF = WRING + WSLOT * NWS;
constexpr int K1_LDS = ONES_OFF + 64;
constexpr int P_STAT = 0;
constexpr int P_CS = 12288;
constexpr int P_ROW = 13312;
constexpr int P_QKV = 16384;
constexpr int QKV_STRIDE = 488;

#define WAIT_V(n) asm volatile("s_waitcnt vmcnt(%0)" ::"n"(n) : "memory")
#define WAIT_L0() asm volatile("s_waitcnt lgkmcnt(0)" ::: "memory")
#define RAW_BARRIER() do { WAIT_L0(); __builtin_amdgcn_s_barrier(); } while (0)

__device__ __forceinline__ float bfr(float f) { return (float)(__bf16)f; }
__device__ __forceinline__ unsigned pack2(float a, float b) {
  bf16x2v v; v[0] = (__bf16)a; v[1] = (__bf16)b; return __builtin_bit_cast(unsigned, v);
}
__device__ __forceinline__ float bflo(unsigned u) { return __builtin_bit_cast(float, u << 16); }
__device__ __forceinline__ float bfhi(unsigned u) { return __builtin_bit_cast(float, u & 0xffff0000u); }
__device__ __forceinline__ float dot2(unsigned a, unsigned b, float c) {
  return __builtin_amdgcn_fdot2_f32_bf16(__builtin_bit_cast(bf16x2v, a), __builtin_bit_cast(bf16x2v, b), c, false);
}
__device__ __forceinline__ void vadd(float& a, float b) { asm("v_add_f32 %0, %0, %1" : "+v"(a) : "v"(b)); }
__device__ __forceinline__ void vsq(float& a, float b) { asm("v_fmac_f32 %0, %1, %1" : "+v"(a) : "v"(b)); }
__device__ __forceinline__ void st16(void* p, u32x4 v) {
  asm volatile("global_store_dwordx4 %0, %1, off sc1\n\ts_nop 1" ::"v"(p), "v"(v) : "memory");
}
__device__ __forceinline__ void st8(void* p, u32x2 v) {
  asm volatile("global_store_dwordx2 %0, %1, off sc1\n\ts_nop 1" ::"v"(p), "v"(v) : "memory");
}
__device__ __forceinline__ void dma16(const void* g, char* lds_uniform) {
  __builtin_amdgcn_global_load_lds((const unsigned*)g, (unsigned*)lds_uniform, 16, 0, 0);
}
__device__ __forceinline__ s16x4 tr_read(const char* p) {
  return __builtin_amdgcn_ds_read_tr16_b64_v4i16((LDS_AS s16x4*)p);
}

#define SB() __builtin_amdgcn_sched_barrier(0)

__device__ __forceinline__ void attention_phase(const char* smem, char* __restrict__ act, int rb, int cb, int wave,
                                                int lane) {
  constexpr int NU = 3;
  const float scale = 0.22360679774997896f * 1.4426950408889634f;
  const int g = lane >> 4, c = lane & 15;
  const int q4 = c >> 2, p4 = c & 3;
  bool okm[8]; int dlv[8];
#pragma unroll
  for (int i = 0; i < 8; ++i) {
    dlv[i] = 16 + c - (16 * (i >> 2) + 4 * g + (i & 3));
    okm[i] = dlv[i] >= 0 && dlv[i] <= 14 && !(dlv[i] & 1);
  }
  const f32x4 z4 = {0.f, 0.f, 0.f, 0.f};
  u32x4 qf[NU], kf0[NU], kf1[NU];
  bf16x8 vf0[NU], vf1[NU];
  float lg[NU][8], mx[NU], sum[NU];
#pragma unroll
  for (int it = 0; it < NU; ++it) {
    const int u = wave + 12 * it;
    const int rt = u >> 2, hl = u & 3;
    const char* base = smem + P_QKV + (16 * rt) * QKV_STRIDE + hl * 40;
    const char* qp = base + (16 + c) * QKV_STRIDE + 16 * g;
    u32x2 qa = *(const u32x2*)qp, qb = *(const u32x2*)(qp + 8);
    const char* kp = base + c * QKV_STRIDE + 160 + 16 * g;
    u32x2 k0a = *(const u32x2*)kp, k0b = *(const u32x2*)(kp + 8);
    u32x2 k1a = *(const u32x2*)(kp + 16 * QKV_STRIDE), k1b = *(const u32x2*)(kp + 16 * QKV_STRIDE + 8);
    const char* vp = base + (4 * g + q4) * QKV_STRIDE + 320 + 8 * p4;
    s16x4 v00 = tr_read(vp), v01 = tr_read(vp + 16 * QKV_STRIDE);
    s16x4 v10 = tr_read(vp + 32), v11 = tr_read(vp + 16 * QKV_STRIDE + 32);
    qf[it][0] = g < 3 ? qa[0] : 0u; qf[it][1] = g < 3 ? qa[1] : 0u; qf[it][2] = g < 2 ? qb[0] : 0u; qf[it][3] = g < 2 ? qb[1] : 0u;
    kf0[it][0] = k0a[0]; kf0[it][1] = k0a[1]; kf0[it][2] = k0b[0]; kf0[it][3] = k0b[1];
    kf1[it][0] = k1a[0]; kf1[it][1] = k1a[1]; kf1[it][2] = k1b[0]; kf1[it][3] = k1b[1];
    vf0[it] = __builtin_shufflevector(v00, v01, 0, 1, 2, 3, 4, 5, 6, 7);
    vf1[it] = __builtin_shufflevector(v10, v11, 0, 1, 2, 3, 4, 5, 6, 7);
  }
  SB();
#pragma unroll
  for (int it = 0; it < NU; ++it) {
    const int u = wave + 12 * it;
    const int sq = rb * 144 + 16 * (u >> 2) + c;
    f32x4 s0 = __builtin_amdgcn_mfma_f32_16x16x32_bf16(__builtin_bit_cast(bf16x8, kf0[it]), __builtin_bit_cast(bf16x8, qf[it]), z4, 0, 0, 0);
    f32x4 s1 = __builtin_amdgcn_mfma_f32_16x16x32_bf16(__builtin_bit_cast(bf16x8, kf1[it]), __builtin_bit_cast(bf16x8, qf[it]), z4, 0, 0, 0);
#pragma unroll
    for (int i = 0; i < 8; ++i) {
      const float d = i < 4 ? s0[i & 3] : s1[i & 3];
      lg[it][i] = (okm[i] && dlv[i] <= sq) ? d * scale : -1e9f;
    }
    mx[it] = fmaxf(fmaxf(fmaxf(lg[it][0], lg[it][1]), fmaxf(lg[it][2], lg[it][3])),
                   fmaxf(fmaxf(lg[it][4], lg[it][5]), fmaxf(lg[it][6], lg[it][7])));
  }
  SB();
#pragma unroll
  for (int it = 0; it < NU; ++it) mx[it] = fmaxf(mx[it], __shfl_xor(mx[it], 16));
  SB();
#pragma unroll
  for (int it = 0; it < NU; ++it) mx[it] = fmaxf(mx[it], __shfl_xor(mx[it], 32));
  SB();
#pragma unroll
  for (int it = 0; it < NU; ++it) {
    float sm = 0.f;
#pragma unroll
    for (int i = 0; i < 8; ++i) { lg[it][i] = __builtin_amdgcn_exp2f(lg[it][i] - mx[it]); sm += lg[it][i]; }
    sum[it] = sm;
  }
  SB();
#pragma unroll
  for (int it = 0; it < NU; ++it) sum[it] += __shfl_xor(sum[it], 16);
  SB();
#pragma unroll
  for (int it = 0; it < NU; ++it) sum[it] += __shfl_xor(sum[it], 32);
  SB();
  u32x2 res0[NU], res1[NU];
#pragma unroll
  for (int it = 0; it < NU; ++it) {
    const float inv = __builtin_amdgcn_rcpf(sum[it]);
    u32x4 pf;
#pragma unroll
    for (int i = 0; i < 4; ++i) pf[i] = pack2(lg[it][2 * i] * inv, lg[it][2 * i + 1] * inv);
    f32x4 o0 = __builtin_amdgcn_mfma_f32_16x16x32_bf16(vf0[it], __builtin_bit_cast(bf16x8, pf), z4, 0, 0, 0);
    f32x4 o1 = __builtin_amdgcn_mfma_f32_16x16x32_bf16(vf1[it], __builtin_bit_cast(bf16x8, pf), z4, 0, 0, 0);
    res0[it][0] = pack2(o0[0], o0[1]); res0[it][1] = pack2(o0[2], o0[3]);
    res1[it][0] = pack2(o1[0], o1[1]); res1[it][1] = pack2(o1[2], o1[3]);
  }
#pragma unroll
  for (int it = 0; it < NU; ++it) {
    const int u = wave + 12 * it;
    const int rt = u >> 2, hl = u & 3;
    const int sq = rb * 144 + 16 * rt + c;
    if (sq < S_) {
      char* obase = act + (size_t)(sq >> 4) * KS2 * 1024 + (sq & 15) * 16;
      {
        const int col = cb * 80 + hl * 20 + 4 * g;
        const int ks = col >> 5, kk = col & 31;
        st8(obase + ks * 1024 + (2 * (kk >> 4) + ((kk >> 2) & 1)) * 256 + ((kk >> 3) & 1) * 8, res0[it]);
      }
      if (g == 0) {
        const int col = cb * 80 + hl * 20 + 16;
        const int ks = col >> 5, kk = col & 31;
        st8(obase + ks * 1024 + (2 * (kk >> 4) + ((kk >> 2) & 1)) * 256 + ((kk >> 3) & 1) * 8, res1[it]);
      }
    }
  }
}

struct Retile { u32x4 r[8]; };
__device__ __forceinline__ int retile_items(int rb2) { return rb2 < 4 ? 120 : 80; }
__device__ __forceinline__ void retile_load(Retile& R, const u16* __restrict__ wo, const u16* __restrict__ w2,
                                            int rb2, int nb, int tid) {
  const int it = tid < retile_items(rb2) ? tid : 0;
  const int ks = (rb2 < 4 ? 3 * rb2 : 12 + 2 * (rb2 - 4)) + it / 40, g = (it % 40) / 10, c8 = nb * 10 + it % 10;
  const int k0 = 32 * ks + 16 * (g >> 1) + 4 * (g & 1);
  const u16* src = (ks < 20 ? wo + (size_t)k0 * D_ : w2 + (size_t)(k0 - D_) * D_) + c8 * 8;
#pragma unroll
  for (int j = 0; j < 8; ++j) R.r[j] = *(const u32x4*)(src + (size_t)(8 * (j >> 2) + (j & 3)) * D_);
}
__device__ __forceinline__ void retile_store(const Retile& R, char* __restrict__ ws, int rb2, int nb, int tid,
                                             char* stage) {
  const int lane = tid & 63, t0 = tid & ~63;
#pragma unroll
  for (int c = 0; c < 8; ++c) {
    u32x4 o;
#pragma unroll
    for (int d = 0; d < 4; ++d)
      o[d] = __builtin_amdgcn_perm(R.r[2 * d + 1][c >> 1], R.r[2 * d][c >> 1], (c & 1) ? 0x07060302u : 0x05040100u);
    *(u32x4*)(stage + lane * 128 + c * 16) = o;
  }
  WAIT_L0();
#pragma unroll
  for (int q = 0; q < 8; ++q) {
    const int tl = q * 8 + (lane >> 3), it = t0 + tl;
    const u32x4 o = *(const u32x4*)(stage + tl * 128 + (lane & 7) * 16);
    if (it < retile_items(rb2)) {
      const int ks = (rb2 < 4 ? 3 * rb2 : 12 + 2 * (rb2 - 4)) + it / 40, g = (it % 40) / 10, c8 = nb * 10 + it % 10;
      st16(ws + WF_OFF + ((size_t)(ks * 40 + (c8 >> 1)) * 64 + g * 16 + (c8 & 1) * 8 + (lane & 7)) * 16, o);
    }
  }
}

__device__ __forceinline__ void phase2(char* smem, const u16* __restrict__ x, char* __restrict__ ws,
                                       u16* __restrict__ out, int kind, int rb, int cb, int rb2, int nb,
                                       const int wave) {
  typedef __attribute__((address_space(1))) unsigned long long gu64;
  int lane = __builtin_amdgcn_mbcnt_hi(~0u, __builtin_amdgcn_mbcnt_lo(~0u, 0u));
  asm volatile("" : "+v"(lane));
  char* stampbuf = ws + (64u << 20) + 512 * 128; (void)stampbuf;
  WAIT_V(0);
  RAW_BARRIER();
  STAMP(0);
  if (wave == 8) {
    gu64* mbox = (gu64*)(ws + MB_OFF);
    int tgt = -1;
    if (lane < 32) {
      if (kind != 2) {
        const int rows = kind == 0 ? 144 : 191;
        const int r_lo = rb * rows, r_hi = (r_lo + rows > S_ ? S_ : r_lo + rows) - 1;
        const int c2 = (r_lo >> 6) + (lane >> 3);
        if (c2 <= (r_hi >> 6)) {
          const int first = (c2 * 64) / rows;
          const int slot = kind == 0 ? (rb - first) * 8 + cb : 16 + (rb - first) * 12 + cb;
          tgt = (c2 * 8 + (lane & 7)) * 128 + slot;
        }
      }
    } else {
      tgt = ((lane - 32) * 8 + nb) * 128 + 40 + rb2;
    }
    if (tgt >= 0) __hip_atomic_store(mbox + tgt, MAGIC, __ATOMIC_RELAXED, __HIP_MEMORY_SCOPE_AGENT);
    gu64* mine = mbox + (rb2 * 8 + nb) * 128;
    const int nq = (rb2 * 64 + 63) / 144 - (rb2 * 64) / 144, nf = (rb2 * 64 + 63) / 191 - (rb2 * 64) / 191;
    const bool exp0 = lane < 16 ? (lane >> 3) <= nq : (lane < 40 ? (lane - 16) / 12 <= nf : true);
    for (unsigned spin = 0; spin < (1u << 22); ++spin) {
      const unsigned long long v0 = __hip_atomic_load(mine + lane, __ATOMIC_RELAXED, __HIP_MEMORY_SCOPE_AGENT);
      const unsigned long long v1 = __hip_atomic_load(mine + 64 + (lane & 7), __ATOMIC_RELAXED, __HIP_MEMORY_SCOPE_AGENT);
      if (__all((!exp0 || v0 == MAGIC) && v1 == MAGIC)) break;
      __builtin_amdgcn_s_sleep(2);
    }
    __hip_atomic_store(mine + lane, 0ull, __ATOMIC_RELAXED, __HIP_MEMORY_SCOPE_AGENT);
    if (lane < 8) __hip_atomic_store(mine + 64 + lane, 0ull, __ATOMIC_RELAXED, __HIP_MEMORY_SCOPE_AGENT);
    __builtin_amdgcn_fence(__ATOMIC_ACQUIRE, "agent");
    WAIT_V(0);
  }
  RAW_BARRIER();
  STAMP(1);

  char* red = smem;
  f32x4 acc[4][5];
  if (wave < 8) {
    const int ks0 = wave < 4 ? 9 * wave : 36 + 8 * (wave - 4);
    const int nst = wave < 4 ? 9 : 8;
    const char* ap0 = ws + ((size_t)(rb2 * 4) * KS2 + ks0) * 1024;
    const char* wp = ws + WF_OFF + ((size_t)ks0 * 40 + nb * 5) * 1024;
    const unsigned lo = lane * 16;
#pragma unroll
    for (int i = 0; i < 4; ++i)
#pragma unroll
      for (int j = 0; j < 5; ++j) acc[i][j] = (f32x4){0.f, 0.f, 0.f, 0.f};
    const char* apu[4];
#pragma unroll
    for (int i = 0; i < 4; ++i) apu[i] = ap0 + (size_t)i * KS2 * 1024;
    char* ring = smem + wave * 15360;
    u32x4 af[3][4];
    auto ld = [&](int buf) {
#pragma unroll
      for (int j = 0; j < 5; ++j) dma16(wp + j * 1024 + lo, ring + buf * 5120 + j * 1024);
      wp += 40960;
#pragma unroll
      for (int i = 0; i < 4; ++i) {
        asm volatile("global_load_dwordx4 %0, %1, %2" : "=v"(af[buf][i]) : "v"(lo), "s"(apu[i]) : "memory");
        apu[i] += 1024;
      }
    };
    auto mm = [&](int buf) {
      SB();
      bf16x8 wf[5];
#pragma unroll
      for (int j = 0; j < 5; ++j) wf[j] = *(const bf16x8*)(ring + buf * 5120 + j * 1024 + lo);
#pragma unroll
      for (int j = 0; j < 5; ++j)
#pragma unroll
        for (int i = 0; i < 4; ++i)
          acc[i][j] = __builtin_amdgcn_mfma_f32_16x16x32_bf16(wf[j], __builtin_bit_cast(bf16x8, af[buf][i]), acc[i][j], 0, 0, 0);
      WAIT_L0();
      SB();
    };
    ld(0); ld(1);
#pragma nounroll
    for (int st = 0; st < 6; st += 3) {
      SB(); ld(2); WAIT_V(18); mm(0);
      SB(); ld(0); WAIT_V(18); mm(1);
      SB(); ld(1); WAIT_V(18); mm(2);
    }
    SB();
    if (nst == 9) ld(2);
    WAIT_V(9); mm(0);
    WAIT_V(0); mm(1);
    if (nst == 9) mm(2);
    lane = __builtin_amdgcn_mbcnt_hi(~0u, __builtin_amdgcn_mbcnt_lo(~0u, 0u));
    asm volatile("" : "+v"(lane));
  }
  STAMP(2);
  RAW_BARRIER();
  STAMP(3);
  if (wave >= 4 && wave < 8) {
#pragma unroll
    for (int i = 0; i < 4; ++i)
#pragma unroll
      for (int j = 0; j < 5; ++j) *(f32x4*)(red + (((wave - 4) * 20 + i * 5 + j) * 64 + lane) * 16) = acc[i][j];
  }
  RAW_BARRIER();
  if (wave < 4) {
#pragma unroll
    for (int i = 0; i < 4; ++i)
#pragma unroll
      for (int j = 0; j < 5; ++j) {
        f32x4* p = (f32x4*)(red + ((wave * 20 + i * 5 + j) * 64 + lane) * 16);
        *p = *p + acc[i][j];
      }
  }
  RAW_BARRIER();
  const int g = lane >> 4, r = lane & 15;
  for (int tl = wave; tl < 20; tl += 12) {
    const int rt = tl / 5, j = tl - rt * 5;
    f32x4 sacc = *(const f32x4*)(red + ((0 * 20 + tl) * 64 + lane) * 16);
#pragma unroll
    for (int w = 1; w < 4; ++w) sacc += *(const f32x4*)(red + ((w * 20 + tl) * 64 + lane) * 16);
    const int row = rb2 * 64 + rt * 16 + r, col = nb * 80 + j * 16 + g * 4;
    u32x2 xv = *(const u32x2*)(x + (size_t)row * D_ + col);
    u32x2 o;
    o[0] = pack2(bflo(xv[0]) + sacc[0], bfhi(xv[0]) + sacc[1]);
    o[1] = pack2(bflo(xv[1]) + sacc[2], bfhi(xv[1]) + sacc[3]);
    *(u32x2*)(out + (size_t)row * D_ + col) = o;
  }
}

template <bool QKV, int MTW>
__device__ __forceinline__ void k1_compute(char* smem, const u16* __restrict__ wa, const u16* __restrict__ wb,
                                           char* __restrict__ act, int rb, int cb, const int wave,
                                           const u16* __restrict__ wo, const u16* __restrict__ w2, int rb2, int nb) {
  char* stampbuf = act + (64u << 20); (void)stampbuf;
  STAMP(0);
  const int tid = threadIdx.x, lane = tid & 63;
  const int wm = wave >> 2, wn = wave & 3;
  const int h = lane >> 5, r32 = lane & 31;
  const int row0 = QKV ? rb * 144 - 16 : rb * 191 - 1;

  const char* wsrc[2];
#pragma unroll
  for (int j = 0; j < 2; ++j) {
    int k = 4 * wave + 2 * j + (lane >> 5);
    int c = (lane & 31) ^ ((k & 3) << 2);
    if (QKV) {
      int cc = c > 29 ? 29 : c;
      int seg = cc / 10, cq = cc - seg * 10;
      wsrc[j] = (const char*)wa + ((size_t)k * 1920 + seg * 640 + cb * 80 + cq * 8) * 2;
    } else {
      int wv = c >> 2, part = (c >> 1) & 1, hf = c & 1;
      wsrc[j] = (const char*)(part ? wb : wa) + ((size_t)k * F_ + cb * 128 + wv * 16 + hf * 8) * 2;
    }
  }
  constexpr int WADV2 = (QKV ? 1920 : F_) * 2 * 64;
  const char* wsrc0 = wsrc[0];
  const int wdelta = (int)(wsrc[1] - wsrc[0]);
  auto dmaW = [&](int j, int slot) {
    dma16(j ? wsrc0 + wdelta : wsrc0, smem + WRING + slot * WSLOT + wave * 2048 + j * 1024);
    if (j) wsrc0 += WADV2;
  };

  const int xoff0 = (wm * 96 + r32) * 128 + ((h ^ ((r32 >> 1) & 7)) * 16);
  const bool onesl = (wm == 0) && (r32 == 0);
  int woff0;
  {
    int blk = (lane >> 4) & 1, q4 = (lane & 15) >> 2, p = lane & 3;
    woff0 = WRING + (8 * h + q4) * 512 + (((8 * wn + 2 * blk + (p >> 1)) ^ (q4 << 2)) * 16) + (p & 1) * 8;
  }

  f32x16 acc[MTW][2];
#pragma unroll
  for (int m = 0; m < MTW; ++m)
#pragma unroll
    for (int i = 0; i < 16; ++i) { acc[m][0][i] = 0.f; acc[m][1][i] = 0.f; }

  bf16x8 xf0[MTW], xf1[MTW], wf0[2], wf1[2];
  auto LX = [&](bf16x8(&xf)[MTW], int m, int xbase, int q) {
    if (m >= MTW) return;
    const int a = xbase + (xoff0 ^ (q * 32));
    if (m == 0) xf[0] = *(const bf16x8*)(smem + (onesl ? ONES_OFF : a));
    else xf[m] = *(const bf16x8*)(smem + a + m * 4096);
  };
  auto LW = [&](bf16x8(&wf)[2], int j, int wbase, int k16) {
    const int o = wbase + (j ? (woff0 ^ 64) : woff0) + k16 * 8192;
    s16x4 a0 = tr_read(smem + o);
    s16x4 a1 = tr_read(smem + o + 2048);
    wf[j] = __builtin_shufflevector(a0, a1, 0, 1, 2, 3, 4, 5, 6, 7);
  };
  auto MF = [&](int i, bf16x8(&xf)[MTW], bf16x8(&wf)[2]) {
    SB();
    if ((i >> 1) < MTW)
      acc[i >> 1][i & 1] = __builtin_amdgcn_mfma_f32_32x32x16_bf16(wf[i & 1], xf[i >> 1], acc[i >> 1][i & 1], 0, 0, 0);
    SB();
  };
  auto sub = [&](bf16x8(&xc)[MTW], bf16x8(&wc)[2], bf16x8(&xn)[MTW], bf16x8(&wn2)[2], int xbn, int qn, int wbn,
                 int k16n, int dslot) {
    MF(0, xc, wc); LW(wn2, 0, wbn, k16n);
    MF(1, xc, wc); LW(wn2, 1, wbn, k16n); LX(xn, 0, xbn, qn);
    MF(2, xc, wc); LX(xn, 1, xbn, qn); LX(xn, 2, xbn, qn);
    MF(3, xc, wc); if (dslot >= 0) dmaW(0, dslot);
    MF(4, xc, wc); if (dslot >= 0) dmaW(1, dslot);
    MF(5, xc, wc);
    SB();
  };

  if (tid == 0) *(u32x4*)(smem + ONES_OFF) = (u32x4){0x3f803f80u, 0x3f803f80u, 0x3f803f80u, 0x3f803f80u};
  if (wave < 2) {
    Retile R;
    retile_load(R, wo, w2, rb2, nb, tid);
    dmaW(0, 0); dmaW(1, 0); dmaW(0, 2); dmaW(1, 2);
    retile_store(R, act, rb2, nb, tid, smem + WRING + 4 * WSLOT + wave * 8192);
    WAIT_V(10);
  } else {
    dmaW(0, 0); dmaW(1, 0); dmaW(0, 2); dmaW(1, 2);
    WAIT_V(2);
  }
  RAW_BARRIER();
  STAMP(1);
  LX(xf0, 0, 0, 0); LX(xf0, 1, 0, 0); LX(xf0, 2, 0, 0); LW(wf0, 0, 0, 0); LW(wf0, 1, 0, 0);

  int ws0 = 0;
  int xs0 = 0;
  auto pair_steps = [&](auto tailc) {
    constexpr int TAIL = decltype(tailc)::value;
    {
      RAW_BARRIER();
      int ws1 = ws0 + 1; if (ws1 >= NWS) ws1 -= NWS;
      int ws4 = ws0 + 4; if (ws4 >= NWS) ws4 -= NWS;
      const int xb = xs0 * XSLOT, wb0 = ws0 * WSLOT, wb1 = ws1 * WSLOT;
      sub(xf0, wf0, xf1, wf1, xb, 1, wb0, 1, TAIL ? -1 : ws4);
      sub(xf1, wf1, xf0, wf0, xb, 2, wb1, 0, -1);
      ws0 = ws1;
    }
    {
      if (TAIL == 0) WAIT_V(2);
      if (TAIL == 1) WAIT_V(0);
      RAW_BARRIER();
      int ws1 = ws0 + 1; if (ws1 >= NWS) ws1 -= NWS;
      int xs1 = xs0 + 1; if (xs1 >= NXS) xs1 -= NXS;
      const int xb = xs0 * XSLOT, xb1 = xs1 * XSLOT, wb0 = ws0 * WSLOT, wb1 = ws1 * WSLOT;
      sub(xf0, wf0, xf1, wf1, xb, 3, wb0, 1, -1);
      sub(xf1, wf1, xf0, wf0, xb1, 0, wb1, 0, -1);
      ws0 = ws1; xs0 = xs1;
    }
  };
  for (int s = 0; s < 16; s += 2) pair_steps(std::integral_constant<int, 0>{});
  pair_steps(std::integral_constant<int, 1>{});
  pair_steps(std::integral_constant<int, 2>{});

  RAW_BARRIER();
  STAMP(2);
  if (onesl) {
#pragma unroll
    for (int j = 0; j < 2; ++j)
#pragma unroll
      for (int q = 0; q < 4; ++q)
        *(f32x4*)(smem + P_CS + ((2 * wn + j) * 32 + 8 * q + 4 * h) * 4) =
            (f32x4){acc[0][j][4 * q], acc[0][j][4 * q + 1], acc[0][j][4 * q + 2], acc[0][j][4 * q + 3]};
  }
  RAW_BARRIER();
  STAMP(3);

  float2 rs[MTW];
#pragma unroll
  for (int m = 0; m < MTW; ++m) rs[m] = ((const float2*)(smem + P_ROW))[(wm * 3 + m) * 32 + r32];

  if (!QKV) {
    const int ks = 20 + cb * 4 + wn;
#pragma unroll
    for (int j = 0; j < 2; ++j) {
      float csv[16];
#pragma unroll
      for (int q = 0; q < 4; ++q) {
        f32x4 v = *(const f32x4*)(smem + P_CS + ((2 * wn + j) * 32 + 8 * q + 4 * h) * 4);
        csv[4 * q] = v[0]; csv[4 * q + 1] = v[1]; csv[4 * q + 2] = v[2]; csv[4 * q + 3] = v[3];
      }
#pragma unroll
      for (int m = 0; m < MTW; ++m) {
        float hv[8];
#pragma unroll
        for (int i = 0; i < 8; ++i) {
          float g = (acc[m][j][i] - rs[m].x * csv[i]) * rs[m].y;
          float u = (acc[m][j][8 + i] - rs[m].x * csv[8 + i]) * rs[m].y;
          hv[i] = g * __builtin_amdgcn_rcpf(1.f + __expf(-g)) * u;
        }
        int grow = row0 + (wm * 3 + m) * 32 + r32;
        if (grow < S_ && !(m == 0 && onesl)) {
          u32x4 o; o[0] = pack2(hv[0], hv[1]); o[1] = pack2(hv[2], hv[3]); o[2] = pack2(hv[4], hv[5]); o[3] = pack2(hv[6], hv[7]);
          st16(act + ((size_t)(grow >> 4) * KS2 + ks) * 1024 + ((2 * j + h) * 16 + (grow & 15)) * 16, o);
        }
      }
    }
  } else {
#pragma unroll
    for (int j = 0; j < 2; ++j) {
#pragma unroll
      for (int q = 0; q < 4; ++q) {
        const int col = (2 * wn + j) * 32 + 8 * q + 4 * h;
        f32x4 cv = *(const f32x4*)(smem + P_CS + col * 4);
#pragma unroll
        for (int m = 0; m < MTW; ++m) {
          float v0 = (acc[m][j][4 * q] - rs[m].x * cv[0]) * rs[m].y, v1 = (acc[m][j][4 * q + 1] - rs[m].x * cv[1]) * rs[m].y;
          float v2 = (acc[m][j][4 * q + 2] - rs[m].x * cv[2]) * rs[m].y, v3 = (acc[m][j][4 * q + 3] - rs[m].x * cv[3]) * rs[m].y;
          u32x2 o; o[0] = pack2(v0, v1); o[1] = pack2(v2, v3);
          if (col < 240) *(u32x2*)(smem + P_QKV + ((wm * 3 + m) * 32 + r32) * QKV_STRIDE + col * 2) = o;
        }
      }
    }
    RAW_BARRIER();
    STAMP(4);
    attention_phase(smem, act, rb, cb, wave, lane);
  }
  STAMP(5);
}

template <bool QKV>
__device__ __forceinline__ void k1_helper(char* smem, const u16* __restrict__ x, const u16* __restrict__ wa,
                                          const u16* __restrict__ wb, char* __restrict__ act, int rb, int cb,
                                          const int wave) {
  constexpr int BMR = QKV ? 160 : 192;
  constexpr int NX = BMR / 32;
  const int lane = threadIdx.x & 63;
  const int hw = wave - 8;
  const int b2 = blockIdx.x & 7; (void)b2;
  const int row0 = QKV ? rb * 144 - 16 : rb * 191 - 1;

  const char* xsrc[NX];
#pragma unroll
  for (int j = 0; j < NX; ++j) {
    int lr = 8 * (hw * NX + j) + (lane >> 3);
    int c = (lane & 7) ^ ((lr >> 1) & 7);
    int gr = row0 + lr; gr = gr < 0 ? 0 : (gr > S_ - 1 ? S_ - 1 : gr);
    xsrc[j] = (const char*)x + (size_t)gr * (D_ * 2) + c * 16;
  }
  const char* wsrc[4];
#pragma unroll
  for (int j = 0; j < 4; ++j) {
    int k = 8 * hw + 2 * j + (lane >> 5);
    int c = (lane & 31) ^ ((k & 3) << 2);
    if (QKV) {
      int cc = c > 29 ? 29 : c;
      int seg = cc / 10, cq = cc - seg * 10;
      wsrc[j] = (const char*)wa + ((size_t)k * 1920 + seg * 640 + cb * 80 + cq * 8) * 2;
    } else {
      int wv = c >> 2, part = (c >> 1) & 1, hf = c & 1;
      wsrc[j] = (const char*)(part ? wb : wa) + ((size_t)k * F_ + cb * 128 + wv * 16 + hf * 8) * 2;
    }
  }
  constexpr int WADV = (QKV ? 1920 : F_) * 2 * 64;
#pragma unroll
  for (int j = 0; j < 4; ++j) wsrc[j] += WADV / 2;
  auto issueW = [&](int slot) {
#pragma unroll
    for (int j = 0; j < 4; ++j) { dma16(wsrc[j], smem + WRING + slot * WSLOT + hw * 4096 + j * 1024); wsrc[j] += WADV; }
  };
  auto issueX = [&](int slot) {
#pragma unroll
    for (int j = 0; j < NX; ++j) { dma16(xsrc[j], smem + slot * XSLOT + (hw * NX + j) * 1024); xsrc[j] += 128; }
  };

  float sx[NX][2], sxx[NX][2];
#pragma unroll
  for (int i = 0; i < NX; ++i) { sx[i][0] = sx[i][1] = 0.f; sxx[i][0] = sxx[i][1] = 0.f; }
  const int soff = hw * NX * 1024 + lane * 16;
  u32x4 sv[NX];
  auto stats_ld = [&](int xbase) {
#pragma unroll
    for (int i = 0; i < NX; ++i) sv[i] = *(const u32x4*)(smem + xbase + soff + i * 1024);
  };
  auto stats_acc = [&](int k) {
    if (k >= 2 * NX) return;
    const int i = k >> 1;
#pragma unroll
    for (int d = (k & 1) * 2; d < (k & 1) * 2 + 2; ++d) {
      float lo = bflo(sv[i][d]), hi = bfhi(sv[i][d]);
      vadd(sx[i][0], lo); vadd(sx[i][1], hi);
      vsq(sxx[i][0], lo); vsq(sxx[i][1], hi);
    }
  };
  auto stats_all = [&](int xbase) {
    stats_ld(xbase);
#pragma unroll
    for (int k = 0; k < 2 * NX; ++k) stats_acc(k);
  };

  issueX(0); issueW(1); issueX(1); issueW(3);
  WAIT_V(8 + NX);
  RAW_BARRIER();
  int ws5 = 0, xs0 = 0;
  for (int s = 0; s < 16; s += 2) {
    WAIT_V(4 + NX); RAW_BARRIER();
    {
      int xs2 = xs0 + 2; if (xs2 >= NXS) xs2 -= NXS;
      stats_ld(xs0 * XSLOT);
#pragma unroll
      for (int j = 0; j < NX; ++j) {
        SB(); dma16(xsrc[j], smem + xs2 * XSLOT + (hw * NX + j) * 1024); xsrc[j] += 128; SB();
        if (j) stats_acc(j - 1);
      }
      stats_acc(NX - 1);
    }
    WAIT_V(4 + NX); RAW_BARRIER();
#pragma unroll
    for (int j = 0; j < 4; ++j) {
      SB(); dma16(wsrc[j], smem + WRING + ws5 * WSLOT + hw * 4096 + j * 1024); wsrc[j] += WADV; SB();
      stats_acc(NX + j);
    }
    stats_acc(NX + 4); stats_acc(NX + 5);
    ws5 += 2; if (ws5 >= NWS) ws5 -= NWS;
    if (++xs0 >= NXS) xs0 = 0;
  }
  WAIT_V(4 + NX); RAW_BARRIER();
  stats_all(xs0 * XSLOT);
  if (++xs0 >= NXS) xs0 = 0;
  WAIT_V(4); RAW_BARRIER();
  WAIT_V(0); RAW_BARRIER();
  stats_all(xs0 * XSLOT);
  RAW_BARRIER();
  float2 rsv[NX];
#pragma unroll
  for (int i = 0; i < NX; ++i) {
    float a = sx[i][0] + sx[i][1], b = sxx[i][0] + sxx[i][1];
    a += __shfl_xor(a, 1); b += __shfl_xor(b, 1);
    a += __shfl_xor(a, 2); b += __shfl_xor(b, 2);
    a += __shfl_xor(a, 4); b += __shfl_xor(b, 4);
    float mean = a * (1.f / D_);
    float var = b * (1.f / D_) - mean * mean; var = var < 0.f ? 0.f : var;
    float mb = bfr(mean);
    float den = bfr(sqrtf(bfr(bfr(var) + 1e-5f)));
    rsv[i] = make_float2(mb, 1.f / den);
  }
  RAW_BARRIER();
#pragma unroll
  for (int i = 0; i < NX; ++i)
    if ((lane & 7) == 0) ((float2*)(smem + P_ROW))[8 * (hw * NX + i) + (lane >> 3)] = rsv[i];
  RAW_BARRIER();
  if (QKV) {
    RAW_BARRIER();
    attention_phase(smem, act, rb, cb, wave, lane);
  }
}

__global__ __launch_bounds__(768, 3) void fused_kernel(const u16* __restrict__ x, const u16* __restrict__ wqkv,
                                                       const u16* __restrict__ wo, const u16* __restrict__ w1,
                                                       const u16* __restrict__ w3, const u16* __restrict__ w2,
                                                       char* __restrict__ ws, u16* __restrict__ out) {
  __shared__ __attribute__((aligned(16))) char smem[K1_LDS];
  const int b = blockIdx.x, xcd = b & 7, idx = b >> 3;
  const int wave = __builtin_amdgcn_readfirstlane(threadIdx.x >> 6);
  const int rb2 = 4 * xcd + (idx >> 3), nb = idx & 7;
  int kind, rb, cb;
  if (idx < 15) {
    kind = 0; rb = idx; cb = xcd;
    if (wave < 4) k1_compute<true, 3>(smem, wqkv, wqkv, ws, rb, cb, wave, wo, w2, rb2, nb);
    else if (wave < 8) k1_compute<true, 2>(smem, wqkv, wqkv, ws, rb, cb, wave, wo, w2, rb2, nb);
    else k1_helper<true>(smem, x, wqkv, wqkv, ws, rb, cb, wave);
  } else {
    const int t = xcd * 17 + (idx - 15);
    if (t < 132) {
      kind = 1; rb = t % 11; cb = t / 11;
      if (wave < 8) k1_compute<false, 3>(smem, w1, w3, ws, rb, cb, wave, wo, w2, rb2, nb);
      else k1_helper<false>(smem, x, w1, w3, ws, rb, cb, wave);
    } else {
      kind = 2; rb = 0; cb = 0;
      if (wave < 2) {
        Retile R;
        retile_load(R, wo, w2, rb2, nb, threadIdx.x);
        retile_store(R, ws, rb2, nb, threadIdx.x, smem + WRING + 4 * WSLOT + wave * 8192);
      }
    }
  }
  phase2(smem, x, ws, out, kind, rb, cb, rb2, nb, wave);
  { char* stampbuf = ws + (64u << 20); (void)stampbuf; STAMP(7); }
}

}

extern "C" void kernel_launch(void* const* d_in, const int* in_sizes, int n_in, void* d_out, int out_size,
                              void* d_ws, size_t ws_size, hipStream_t stream) {
  const u16* x = (const u16*)d_in[0];
  const u16* wqkv = (const u16*)d_in[1];
  const u16* wo = (const u16*)d_in[2];
  const u16* w1 = (const u16*)d_in[4];
  const u16* w3 = (const u16*)d_in[5];
  const u16* w2 = (const u16*)d_in[6];
  fused_kernel<<<256, 768, 0, stream>>>(x, wqkv, wo, w1, w3, w2, (char*)d_ws, (u16*)d_out);
}
```

```cpp
#include <hip/hip_runtime.h>
#include <hip/hip_bf16.h>
#include <stdint.h>
#include <type_traits>

typedef __attribute__((ext_vector_type(8))) short bf16x8;
typedef __attribute__((ext_vector_type(4))) short s16x4;
typedef __attribute__((ext_vector_type(2))) __bf16 bf16x2v;
typedef __attribute__((ext_vector_type(4))) float f32x4;
typedef __attribute__((ext_vector_type(16))) float f32x16;
typedef __attribute__((ext_vector_type(4))) unsigned u32x4;
typedef __attribute__((ext_vector_type(2))) unsigned u32x2;
typedef unsigned short u16;
#define LDS_AS __attribute__((address_space(3)))

#define STAMP(i) do { } while (0)

namespace {

constexpr int S_ = 2048, D_ = 640, F_ = 1536;
constexpr int KS2 = 68;
constexpr size_t ACT_BYTES = (size_t)128 * KS2 * 1024;
constexpr size_t WF_OFF = ACT_BYTES;

constexpr int XSLOT = 24576, NXS = 3;
constexpr int WSLOT = 16384, NWS = 5;
constexpr int WRING = XSLOT * NXS;
constexpr int ONES_OFF = WRING + WSLOT * NWS;
constexpr int K1_LDS = ONES_OFF + 64;
constexpr int P_STAT = 0;
constexpr int P_CS = 12288;
constexpr int P_ROW = 13312;
constexpr int P_QKV = 16384;
constexpr int QKV_STRIDE = 488;

#define WAIT_V(n) asm volatile("s_waitcnt vmcnt(%0)" ::"n"(n) : "memory")
#define WAIT_L0() asm volatile("s_waitcnt lgkmcnt(0)" ::: "memory")
#define RAW_BARRIER() do { WAIT_L0(); __builtin_amdgcn_s_barrier(); } while (0)

__device__ __forceinline__ float bfr(float f) { return (float)(__bf16)f; }
__device__ __forceinline__ unsigned pack2(float a, float b) {
  bf16x2v v; v[0] = (__bf16)a; v[1] = (__bf16)b; return __builtin_bit_cast(unsigned, v);
}
__device__ __forceinline__ float bflo(unsigned u) { return __builtin_bit_cast(float, u << 16); }
__device__ __forceinline__ float bfhi(unsigned u) { return __builtin_bit_cast(float, u & 0xffff0000u); }
__device__ __forceinline__ float dot2(unsigned a, unsigned b, float c) {
  return __builtin_amdgcn_fdot2_f32_bf16(__builtin_bit_cast(bf16x2v, a), __builtin_bit_cast(bf16x2v, b), c, false);
}
__device__ __forceinline__ void vadd(float& a, float b) { asm("v_add_f32 %0, %0, %1" : "+v"(a) : "v"(b)); }
__device__ __forceinline__ void vsq(float& a, float b) { asm("v_fmac_f32 %0, %1, %1" : "+v"(a) : "v"(b)); }
__device__ __forceinline__ void st16(void* p, u32x4 v) {
  asm volatile("global_store_dwordx4 %0, %1, off sc1\n\ts_nop 1" ::"v"(p), "v"(v) : "memory");
}
__device__ __forceinline__ void st8(void* p, u32x2 v) {
  asm volatile("global_store_dwordx2 %0, %1, off sc1\n\ts_nop 1" ::"v"(p), "v"(v) : "memory");
}
__device__ __forceinline__ void dma16(const void* g, char* lds_uniform) {
  __builtin_amdgcn_global_load_lds((const unsigned*)g, (unsigned*)lds_uniform, 16, 0, 0);
}
__device__ __forceinline__ s16x4 tr_read(const char* p) {
  return __builtin_amdgcn_ds_read_tr16_b64_v4i16((LDS_AS s16x4*)p);
}

#define SB() __builtin_amdgcn_sched_barrier(0)

__device__ __forceinline__ void attention_phase(const char* smem, char* __restrict__ act, int rb, int cb, int wave,
                                                int lane) {
  constexpr int NU = 3;
  const float scale = 0.22360679774997896f * 1.4426950408889634f;
  const int g = lane >> 4, c = lane & 15;
  const int q4 = c >> 2, p4 = c & 3;
  bool okm[8]; int dlv[8];
#pragma unroll
  for (int i = 0; i < 8; ++i) {
    dlv[i] = 16 + c - (16 * (i >> 2) + 4 * g + (i & 3));
    okm[i] = dlv[i] >= 0 && dlv[i] <= 14 && !(dlv[i] & 1);
  }
  const f32x4 z4 = {0.f, 0.f, 0.f, 0.f};
  u32x4 qf[NU], kf0[NU], kf1[NU];
  bf16x8 vf0[NU], vf1[NU];
  float lg[NU][8], mx[NU], sum[NU];
#pragma unroll
  for (int it = 0; it < NU; ++it) {
    const int u = wave + 12 * it;
    const int rt = u >> 2, hl = u & 3;
    const char* base = smem + P_QKV + (16 * rt) * QKV_STRIDE + hl * 40;
    const char* qp = base + (16 + c) * QKV_STRIDE + 16 * g;
    u32x2 qa = *(const u32x2*)qp, qb = *(const u32x2*)(qp + 8);
    const char* kp = base + c * QKV_STRIDE + 160 + 16 * g;
    u32x2 k0a = *(const u32x2*)kp, k0b = *(const u32x2*)(kp + 8);
    u32x2 k1a = *(const u32x2*)(kp + 16 * QKV_STRIDE), k1b = *(const u32x2*)(kp + 16 * QKV_STRIDE + 8);
    const char* vp = base + (4 * g + q4) * QKV_STRIDE + 320 + 8 * p4;
    s16x4 v00 = tr_read(vp), v01 = tr_read(vp + 16 * QKV_STRIDE);
    s16x4 v10 = tr_read(vp + 32), v11 = tr_read(vp + 16 * QKV_STRIDE + 32);
    qf[it][0] = g < 3 ? qa[0] : 0u; qf[it][1] = g < 3 ? qa[1] : 0u; qf[it][2] = g < 2 ? qb[0] : 0u; qf[it][3] = g < 2 ? qb[1] : 0u;
    kf0[it][0] = k0a[0]; kf0[it][1] = k0a[1]; kf0[it][2] = k0b[0]; kf0[it][3] = k0b[1];
    kf1[it][0] = k1a[0]; kf1[it][1] = k1a[1]; kf1[it][2] = k1b[0]; kf1[it][3] = k1b[1];
    vf0[it] = __builtin_shufflevector(v00, v01, 0, 1, 2, 3, 4, 5, 6, 7);
    vf1[it] = __builtin_shufflevector(v10, v11, 0, 1, 2, 3, 4, 5, 6, 7);
  }
  SB();
#pragma unroll
  for (int it = 0; it < NU; ++it) {
    const int u = wave + 12 * it;
    const int sq = rb * 144 + 16 * (u >> 2) + c;
    f32x4 s0 = __builtin_amdgcn_mfma_f32_16x16x32_bf16(__builtin_bit_cast(bf16x8, kf0[it]), __builtin_bit_cast(bf16x8, qf[it]), z4, 0, 0, 0);
    f32x4 s1 = __builtin_amdgcn_mfma_f32_16x16x32_bf16(__builtin_bit_cast(bf16x8, kf1[it]), __builtin_bit_cast(bf16x8, qf[it]), z4, 0, 0, 0);
#pragma unroll
    for (int i = 0; i < 8; ++i) {
      const float d = i < 4 ? s0[i & 3] : s1[i & 3];
      lg[it][i] = (okm[i] && dlv[i] <= sq) ? d * scale : -1e9f;
    }
    mx[it] = fmaxf(fmaxf(fmaxf(lg[it][0], lg[it][1]), fmaxf(lg[it][2], lg[it][3])),
                   fmaxf(fmaxf(lg[it][4], lg[it][5]), fmaxf(lg[it][6], lg[it][7])));
  }
  SB();
#pragma unroll
  for (int it = 0; it < NU; ++it) mx[it] = fmaxf(mx[it], __shfl_xor(mx[it], 16));
  SB();
#pragma unroll
  for (int it = 0; it < NU; ++it) mx[it] = fmaxf(mx[it], __shfl_xor(mx[it], 32));
  SB();
#pragma unroll
  for (int it = 0; it < NU; ++it) {
    float sm = 0.f;
#pragma unroll
    for (int i = 0; i < 8; ++i) { lg[it][i] = __builtin_amdgcn_exp2f(lg[it][i] - mx[it]); sm += lg[it][i]; }
    sum[it] = sm;
  }
  SB();
#pragma unroll
  for (int it = 0; it < NU; ++it) sum[it] += __shfl_xor(sum[it], 16);
  SB();
#pragma unroll
  for (int it = 0; it < NU; ++it) sum[it] += __shfl_xor(sum[it], 32);
  SB();
  u32x2 res0[NU], res1[NU];
#pragma unroll
  for (int it = 0; it < NU; ++it) {
    const float inv = __builtin_amdgcn_rcpf(sum[it]);
    u32x4 pf;
#pragma unroll
    for (int i = 0; i < 4; ++i) pf[i] = pack2(lg[it][2 * i] * inv, lg[it][2 * i + 1] * inv);
    f32x4 o0 = __builtin_amdgcn_mfma_f32_16x16x32_bf16(vf0[it], __builtin_bit_cast(bf16x8, pf), z4, 0, 0, 0);
    f32x4 o1 = __builtin_amdgcn_mfma_f32_16x16x32_bf16(vf1[it], __builtin_bit_cast(bf16x8, pf), z4, 0, 0, 0);
    res0[it][0] = pack2(o0[0], o0[1]); res0[it][1] = pack2(o0[2], o0[3]);
    res1[it][0] = pack2(o1[0], o1[1]); res1[it][1] = pack2(o1[2], o1[3]);
  }
#pragma unroll
  for (int it = 0; it < NU; ++it) {
    const int u = wave + 12 * it;
    const int rt = u >> 2, hl = u & 3;
    const int sq = rb * 144 + 16 * rt + c;
    if (sq < S_) {
      char* obase = act + (size_t)(sq >> 4) * KS2 * 1024 + (sq & 15) * 16;
      {
        const int col = cb * 80 + hl * 20 + 4 * g;
        const int ks = col >> 5, kk = col & 31;
        st8(obase + ks * 1024 + (2 * (kk >> 4) + ((kk >> 2) & 1)) * 256 + ((kk >> 3) & 1) * 8, res0[it]);
      }
      if (g == 0) {
        const int col = cb * 80 + hl * 20 + 16;
        const int ks = col >> 5, kk = col & 31;
        st8(obase + ks * 1024 + (2 * (kk >> 4) + ((kk >> 2) & 1)) * 256 + ((kk >> 3) & 1) * 8, res1[it]);
      }
    }
  }
}

template <bool QKV, int MTW>
__device__ __forceinline__ void k1_compute(char* smem, const u16* __restrict__ wa, const u16* __restrict__ wb,
                                           char* __restrict__ act, int rb, int cb, const int wave) {
  char* stampbuf = act + (64u << 20); (void)stampbuf;
  STAMP(0);
  const int tid = threadIdx.x, lane = tid & 63;
  const int wm = wave >> 2, wn = wave & 3;
  const int h = lane >> 5, r32 = lane & 31;
  const int row0 = QKV ? rb * 144 - 16 : rb * 191 - 1;

  const char* wsrc[2];
#pragma unroll
  for (int j = 0; j < 2; ++j) {
    int k = 4 * wave + 2 * j + (lane >> 5);
    int c = (lane & 31) ^ ((k & 3) << 2);
    if (QKV) {
      int cc = c > 29 ? 29 : c;
      int seg = cc / 10, cq = cc - seg * 10;
      wsrc[j] = (const char*)wa + ((size_t)k * 1920 + seg * 640 + cb * 80 + cq * 8) * 2;
    } else {
      int wv = c >> 2, part = (c >> 1) & 1, hf = c & 1;
      wsrc[j] = (const char*)(part ? wb : wa) + ((size_t)k * F_ + cb * 128 + wv * 16 + hf * 8) * 2;
    }
  }
  constexpr int WADV2 = (QKV ? 1920 : F_) * 2 * 64;
  const char* wsrc0 = wsrc[0];
  const int wdelta = (int)(wsrc[1] - wsrc[0]);
  auto dmaW = [&](int j, int slot) {
    dma16(j ? wsrc0 + wdelta : wsrc0, smem + WRING + slot * WSLOT + wave * 2048 + j * 1024);
    if (j) wsrc0 += WADV2;
  };

  const int xoff0 = (wm * 96 + r32) * 128 + ((h ^ ((r32 >> 1) & 7)) * 16);
  const bool onesl = (wm == 0) && (r32 == 0);
  int woff0;
  {
    int blk = (lane >> 4) & 1, q4 = (lane & 15) >> 2, p = lane & 3;
    woff0 = WRING + (8 * h + q4) * 512 + (((8 * wn + 2 * blk + (p >> 1)) ^ (q4 << 2)) * 16) + (p & 1) * 8;
  }

  f32x16 acc[MTW][2];
#pragma unroll
  for (int m = 0; m < MTW; ++m)
#pragma unroll
    for (int i = 0; i < 16; ++i) { acc[m][0][i] = 0.f; acc[m][1][i] = 0.f; }

  bf16x8 xf0[MTW], xf1[MTW], wf0[2], wf1[2];
  auto LX = [&](bf16x8(&xf)[MTW], int m, int xbase, int q) {
    if (m >= MTW) return;
    const int a = xbase + (xoff0 ^ (q * 32));
    if (m == 0) xf[0] = *(const bf16x8*)(smem + (onesl ? ONES_OFF : a));
    else xf[m] = *(const bf16x8*)(smem + a + m * 4096);
  };
  auto LW = [&](bf16x8(&wf)[2], int j, int wbase, int k16) {
    const int o = wbase + (j ? (woff0 ^ 64) : woff0) + k16 * 8192;
    s16x4 a0 = tr_read(smem + o);
    s16x4 a1 = tr_read(smem + o + 2048);
    wf[j] = __builtin_shufflevector(a0, a1, 0, 1, 2, 3, 4, 5, 6, 7);
  };
  auto MF = [&](int i, bf16x8(&xf)[MTW], bf16x8(&wf)[2]) {
    SB();
    if ((i >> 1) < MTW)
      acc[i >> 1][i & 1] = __builtin_amdgcn_mfma_f32_32x32x16_bf16(wf[i & 1], xf[i >> 1], acc[i >> 1][i & 1], 0, 0, 0);
    SB();
  };
  auto sub = [&](bf16x8(&xc)[MTW], bf16x8(&wc)[2], bf16x8(&xn)[MTW], bf16x8(&wn2)[2], int xbn, int qn, int wbn,
                 int k16n, int dslot) {
    MF(0, xc, wc); LW(wn2, 0, wbn, k16n);
    MF(1, xc, wc); LW(wn2, 1, wbn, k16n); LX(xn, 0, xbn, qn);
    MF(2, xc, wc); LX(xn, 1, xbn, qn); LX(xn, 2, xbn, qn);
    MF(3, xc, wc); if (dslot >= 0) dmaW(0, dslot);
    MF(4, xc, wc); if (dslot >= 0) dmaW(1, dslot);
    MF(5, xc, wc);
    SB();
  };

  if (tid == 0) *(u32x4*)(smem + ONES_OFF) = (u32x4){0x3f803f80u, 0x3f803f80u, 0x3f803f80u, 0x3f803f80u};
  dmaW(0, 0); dmaW(1, 0); dmaW(0, 2); dmaW(1, 2);
  WAIT_V(2);
  RAW_BARRIER();
  STAMP(1);
  LX(xf0, 0, 0, 0); LX(xf0, 1, 0, 0); LX(xf0, 2, 0, 0); LW(wf0, 0, 0, 0); LW(wf0, 1, 0, 0);

  int ws0 = 0;
  int xs0 = 0;
  auto pair_steps = [&](auto tailc) {
    constexpr int TAIL = decltype(tailc)::value;
    {
      RAW_BARRIER();
      int ws1 = ws0 + 1; if (ws1 >= NWS) ws1 -= NWS;
      int ws4 = ws0 + 4; if (ws4 >= NWS) ws4 -= NWS;
      const int xb = xs0 * XSLOT, wb0 = ws0 * WSLOT, wb1 = ws1 * WSLOT;
      sub(xf0, wf0, xf1, wf1, xb, 1, wb0, 1, TAIL ? -1 : ws4);
      sub(xf1, wf1, xf0, wf0, xb, 2, wb1, 0, -1);
      ws0 = ws1;
    }
    {
      if (TAIL == 0) WAIT_V(2);
      if (TAIL == 1) WAIT_V(0);
      RAW_BARRIER();
      int ws1 = ws0 + 1; if (ws1 >= NWS) ws1 -= NWS;
      int xs1 = xs0 + 1; if (xs1 >= NXS) xs1 -= NXS;
      const int xb = xs0 * XSLOT, xb1 = xs1 * XSLOT, wb0 = ws0 * WSLOT, wb1 = ws1 * WSLOT;
      sub(xf0, wf0, xf1, wf1, xb, 3, wb0, 1, -1);
      sub(xf1, wf1, xf0, wf0, xb1, 0, wb1, 0, -1);
      ws0 = ws1; xs0 = xs1;
    }
  };
  for (int s = 0; s < 16; s += 2) pair_steps(std::integral_constant<int, 0>{});
  pair_steps(std::integral_constant<int, 1>{});
  pair_steps(std::integral_constant<int, 2>{});

  RAW_BARRIER();
  STAMP(2);
  if (onesl) {
#pragma unroll
    for (int j = 0; j < 2; ++j)
#pragma unroll
      for (int q = 0; q < 4; ++q)
        *(f32x4*)(smem + P_CS + ((2 * wn + j) * 32 + 8 * q + 4 * h) * 4) =
            (f32x4){acc[0][j][4 * q], acc[0][j][4 * q + 1], acc[0][j][4 * q + 2], acc[0][j][4 * q + 3]};
  }
  RAW_BARRIER();
  STAMP(3);

  float2 rs[MTW];
#pragma unroll
  for (int m = 0; m < MTW; ++m) rs[m] = ((const float2*)(smem + P_ROW))[(wm * 3 + m) * 32 + r32];

  if (!QKV) {
    const int ks = 20 + cb * 4 + wn;
#pragma unroll
    for (int j = 0; j < 2; ++j) {
      float csv[16];
#pragma unroll
      for (int q = 0; q < 4; ++q) {
        f32x4 v = *(const f32x4*)(smem + P_CS + ((2 * wn + j) * 32 + 8 * q + 4 * h) * 4);
        csv[4 * q] = v[0]; csv[4 * q + 1] = v[1]; csv[4 * q + 2] = v[2]; csv[4 * q + 3] = v[3];
      }
#pragma unroll
      for (int m = 0; m < MTW; ++m) {
        float hv[8];
#pragma unroll
        for (int i = 0; i < 8; ++i) {
          float g = (acc[m][j][i] - rs[m].x * csv[i]) * rs[m].y;
          float u = (acc[m][j][8 + i] - rs[m].x * csv[8 + i]) * rs[m].y;
          hv[i] = g * __builtin_amdgcn_rcpf(1.f + __expf(-g)) * u;
        }
        int grow = row0 + (wm * 3 + m) * 32 + r32;
        if (grow < S_ && !(m == 0 && onesl)) {
          u32x4 o; o[0] = pack2(hv[0], hv[1]); o[1] = pack2(hv[2], hv[3]); o[2] = pack2(hv[4], hv[5]); o[3] = pack2(hv[6], hv[7]);
          st16(act + ((size_t)(grow >> 4) * KS2 + ks) * 1024 + ((2 * j + h) * 16 + (grow & 15)) * 16, o);
        }
      }
    }
  } else {
#pragma unroll
    for (int j = 0; j < 2; ++j) {
#pragma unroll
      for (int q = 0; q < 4; ++q) {
        const int col = (2 * wn + j) * 32 + 8 * q + 4 * h;
        f32x4 cv = *(const f32x4*)(smem + P_CS + col * 4);
#pragma unroll
        for (int m = 0; m < MTW; ++m) {
          float v0 = (acc[m][j][4 * q] - rs[m].x * cv[0]) * rs[m].y, v1 = (acc[m][j][4 * q + 1] - rs[m].x * cv[1]) * rs[m].y;
          float v2 = (acc[m][j][4 * q + 2] - rs[m].x * cv[2]) * rs[m].y, v3 = (acc[m][j][4 * q + 3] - rs[m].x * cv[3]) * rs[m].y;
          u32x2 o; o[0] = pack2(v0, v1); o[1] = pack2(v2, v3);
          if (col < 240) *(u32x2*)(smem + P_QKV + ((wm * 3 + m) * 32 + r32) * QKV_STRIDE + col * 2) = o;
        }
      }
    }
    RAW_BARRIER();
    STAMP(4);
    attention_phase(smem, act, rb, cb, wave, lane);
  }
  STAMP(5);
}

template <bool QKV>
__device__ __forceinline__ void k1_helper(char* smem, const u16* __restrict__ x, const u16* __restrict__ wa,
                                          const u16* __restrict__ wb, char* __restrict__ act, int rb, int cb,
                                          const int wave, const u16* __restrict__ wo, const u16* __restrict__ w2,
                                          int t) {
  constexpr int BMR = QKV ? 160 : 192;
  constexpr int NX = BMR / 32;
  const int lane = threadIdx.x & 63;
  const int hw = wave - 8;
  const int b2 = blockIdx.x & 7; (void)b2;
  const int row0 = QKV ? rb * 144 - 16 : rb * 191 - 1;

  const int hid = hw * 64 + lane;
  const int item = t * 165 + hid;
  const bool doit = !QKV && hid < 165 && item < KS2 * 4 * 80;
  u32x4 r[8];
  if (!QKV) {
    const int it2 = doit ? item : 0;
    const int c8 = it2 % 80, kg = it2 / 80, g = kg & 3, ks = kg >> 2;
    const int k0 = 32 * ks + 16 * (g >> 1) + 4 * (g & 1);
    const u16* src = (ks < 20 ? wo + (size_t)k0 * D_ : w2 + (size_t)(k0 - D_) * D_) + c8 * 8;
#pragma unroll
    for (int j = 0; j < 8; ++j) r[j] = *(const u32x4*)(src + (size_t)(8 * (j >> 2) + (j & 3)) * D_);
  }

  const char* xsrc[NX];
#pragma unroll
  for (int j = 0; j < NX; ++j) {
    int lr = 8 * (hw * NX + j) + (lane >> 3);
    int c = (lane & 7) ^ ((lr >> 1) & 7);
    int gr = row0 + lr; gr = gr < 0 ? 0 : (gr > S_ - 1 ? S_ - 1 : gr);
    xsrc[j] = (const char*)x + (size_t)gr * (D_ * 2) + c * 16;
  }
  const char* wsrc[4];
#pragma unroll
  for (int j = 0; j < 4; ++j) {
    int k = 8 * hw + 2 * j + (lane >> 5);
    int c = (lane & 31) ^ ((k & 3) << 2);
    if (QKV) {
      int cc = c > 29 ? 29 : c;
      int seg = cc / 10, cq = cc - seg * 10;
      wsrc[j] = (const char*)wa + ((size_t)k * 1920 + seg * 640 + cb * 80 + cq * 8) * 2;
    } else {
      int wv = c >> 2, part = (c >> 1) & 1, hf = c & 1;
      wsrc[j] = (const char*)(part ? wb : wa) + ((size_t)k * F_ + cb * 128 + wv * 16 + hf * 8) * 2;
    }
  }
  constexpr int WADV = (QKV ? 1920 : F_) * 2 * 64;
#pragma unroll
  for (int j = 0; j < 4; ++j) wsrc[j] += WADV / 2;
  auto issueW = [&](int slot) {
#pragma unroll
    for (int j = 0; j < 4; ++j) { dma16(wsrc[j], smem + WRING + slot * WSLOT + hw * 4096 + j * 1024); wsrc[j] += WADV; }
  };
  auto issueX = [&](int slot) {
#pragma unroll
    for (int j = 0; j < NX; ++j) { dma16(xsrc[j], smem + slot * XSLOT + (hw * NX + j) * 1024); xsrc[j] += 128; }
  };

  float sx[NX][2], sxx[NX][2];
#pragma unroll
  for (int i = 0; i < NX; ++i) { sx[i][0] = sx[i][1] = 0.f; sxx[i][0] = sxx[i][1] = 0.f; }
  const int soff = hw * NX * 1024 + lane * 16;
  u32x4 sv[NX];
  auto stats_ld = [&](int xbase) {
#pragma unroll
    for (int i = 0; i < NX; ++i) sv[i] = *(const u32x4*)(smem + xbase + soff + i * 1024);
  };
  auto stats_acc = [&](int k) {
    if (k >= 2 * NX) return;
    const int i = k >> 1;
#pragma unroll
    for (int d = (k & 1) * 2; d < (k & 1) * 2 + 2; ++d) {
      float lo = bflo(sv[i][d]), hi = bfhi(sv[i][d]);
      vadd(sx[i][0], lo); vadd(sx[i][1], hi);
      vsq(sxx[i][0], lo); vsq(sxx[i][1], hi);
    }
  };
  auto stats_all = [&](int xbase) {
    stats_ld(xbase);
#pragma unroll
    for (int k = 0; k < 2 * NX; ++k) stats_acc(k);
  };

  issueX(0); issueW(1); issueX(1); issueW(3);
  WAIT_V(8 + NX);
  RAW_BARRIER();
  int ws5 = 0, xs0 = 0;
  for (int s = 0; s < 16; s += 2) {
    WAIT_V(4 + NX); RAW_BARRIER();
    {
      int xs2 = xs0 + 2; if (xs2 >= NXS) xs2 -= NXS;
      stats_ld(xs0 * XSLOT);
#pragma unroll
      for (int j = 0; j < NX; ++j) {
        SB(); dma16(xsrc[j], smem + xs2 * XSLOT + (hw * NX + j) * 1024); xsrc[j] += 128; SB();
        if (j) stats_acc(j - 1);
      }
      stats_acc(NX - 1);
    }
    WAIT_V(4 + NX); RAW_BARRIER();
#pragma unroll
    for (int j = 0; j < 4; ++j) {
      SB(); dma16(wsrc[j], smem + WRING + ws5 * WSLOT + hw * 4096 + j * 1024); wsrc[j] += WADV; SB();
      stats_acc(NX + j);
    }
    stats_acc(NX + 4); stats_acc(NX + 5);
    ws5 += 2; if (ws5 >= NWS) ws5 -= NWS;
    if (++xs0 >= NXS) xs0 = 0;
  }
  WAIT_V(4 + NX); RAW_BARRIER();
  stats_all(xs0 * XSLOT);
  if (++xs0 >= NXS) xs0 = 0;
  WAIT_V(4); RAW_BARRIER();
  WAIT_V(0); RAW_BARRIER();
  stats_all(xs0 * XSLOT);
  RAW_BARRIER();
  float2 rsv[NX];
#pragma unroll
  for (int i = 0; i < NX; ++i) {
    float a = sx[i][0] + sx[i][1], b = sxx[i][0] + sxx[i][1];
    a += __shfl_xor(a, 1); b += __shfl_xor(b, 1);
    a += __shfl_xor(a, 2); b += __shfl_xor(b, 2);
    a += __shfl_xor(a, 4); b += __shfl_xor(b, 4);
    float mean = a * (1.f / D_);
    float var = b * (1.f / D_) - mean * mean; var = var < 0.f ? 0.f : var;
    float mb = bfr(mean);
    float den = bfr(sqrtf(bfr(bfr(var) + 1e-5f)));
    rsv[i] = make_float2(mb, 1.f / den);
  }
  RAW_BARRIER();
#pragma unroll
  for (int i = 0; i < NX; ++i)
    if ((lane & 7) == 0) ((float2*)(smem + P_ROW))[8 * (hw * NX + i) + (lane >> 3)] = rsv[i];
  RAW_BARRIER();
  if (!QKV) {
    if (doit) {
      const int c8 = item % 80, kg = item / 80, g = kg & 3, ks2 = kg >> 2;
      char* dst = act + WF_OFF + ((size_t)(ks2 * 40 + (c8 >> 1)) * 64 + g * 16 + (c8 & 1) * 8) * 16;
#pragma unroll
      for (int c = 0; c < 8; ++c) {
        u32x4 o;
#pragma unroll
        for (int d = 0; d < 4; ++d)
          o[d] = __builtin_amdgcn_perm(r[2 * d + 1][c >> 1], r[2 * d][c >> 1], (c & 1) ? 0x07060302u : 0x05040100u);
        *(u32x4*)(dst + c * 16) = o;
      }
    }
  } else {
    RAW_BARRIER();
    attention_phase(smem, act, rb, cb, wave, lane);
  }
}

__global__ __launch_bounds__(768, 3) void k1_kernel(const u16* __restrict__ x, const u16* __restrict__ wqkv,
                                                    const u16* __restrict__ wo, const u16* __restrict__ w1,
                                                    const u16* __restrict__ w3, const u16* __restrict__ w2,
                                                    char* __restrict__ ws) {
  __shared__ __attribute__((aligned(16))) char smem[K1_LDS];
  const int b = blockIdx.x, xcd = b & 7, idx = b >> 3;
  const int wave = __builtin_amdgcn_readfirstlane(threadIdx.x >> 6);
  if (idx < 15) {
    if (wave < 4) k1_compute<true, 3>(smem, wqkv, wqkv, ws, idx, xcd, wave);
    else if (wave < 8) k1_compute<true, 2>(smem, wqkv, wqkv, ws, idx, xcd, wave);
    else k1_helper<true>(smem, x, wqkv, wqkv, ws, idx, xcd, wave, wo, w2, 0);
  } else {
    const int t = xcd * 17 + (idx - 15);
    if (t >= 132) return;
    if (wave < 8) k1_compute<false, 3>(smem, w1, w3, ws, t % 11, t / 11, wave);
    else k1_helper<false>(smem, x, w1, w3, ws, t % 11, t / 11, wave, wo, w2, t);
  }
}

__global__ __launch_bounds__(256, 1) void k2_kernel(const u16* __restrict__ x, const char* __restrict__ ws,
                                                    u16* __restrict__ out) {
  __shared__ __attribute__((aligned(16))) char red[4 * 20 * 1024];
  const int b = blockIdx.x, xcd = b & 7, idx = b >> 3;
  const int rb = 4 * xcd + (idx >> 3), nb = idx & 7;
  STAMP(0);
  const int lane = threadIdx.x & 63;
  const int wave = __builtin_amdgcn_readfirstlane(threadIdx.x >> 6);
  const char* ap = ws + ((size_t)(rb * 4) * KS2 + 17 * wave) * 1024 + lane * 16;
  const char* wp = ws + WF_OFF + ((size_t)(17 * wave) * 40 + nb * 5) * 1024 + lane * 16;
  constexpr int DEP = 6;
  bf16x8 af[DEP][4], wf[DEP][5];
  f32x4 acc[4][5];
#pragma unroll
  for (int i = 0; i < 4; ++i)
#pragma unroll
    for (int j = 0; j < 5; ++j) acc[i][j] = (f32x4){0.f, 0.f, 0.f, 0.f};
  auto ld = [&](int st, int buf) {
#pragma unroll
    for (int i = 0; i < 4; ++i) af[buf][i] = *(const bf16x8*)(ap + (size_t)i * KS2 * 1024 + st * 1024);
#pragma unroll
    for (int j = 0; j < 5; ++j) wf[buf][j] = *(const bf16x8*)(wp + (size_t)st * 40960 + j * 1024);
  };
#pragma unroll
  for (int st = 0; st < DEP - 1; ++st) ld(st, st);
#pragma unroll
  for (int st = 0; st < 17; ++st) {
    if (st + DEP - 1 < 17) ld(st + DEP - 1, (st + DEP - 1) % DEP);
    __builtin_amdgcn_sched_barrier(0);
#pragma unroll
    for (int i = 0; i < 4; ++i)
#pragma unroll
      for (int j = 0; j < 5; ++j)
        acc[i][j] = __builtin_amdgcn_mfma_f32_16x16x32_bf16(wf[st % DEP][j], af[st % DEP][i], acc[i][j], 0, 0, 0);
    __builtin_amdgcn_sched_barrier(0);
  }
  STAMP(2);
  const int g = lane >> 4, r = lane & 15;
  const int row = rb * 64 + wave * 16 + r;
  u32x2 xres[5];
#pragma unroll
  for (int j = 0; j < 5; ++j) xres[j] = *(const u32x2*)(x + (size_t)row * D_ + nb * 80 + j * 16 + g * 4);
#pragma unroll
  for (int i = 0; i < 4; ++i)
#pragma unroll
    for (int j = 0; j < 5; ++j) *(f32x4*)(red + ((wave * 20 + i * 5 + j) * 64 + lane) * 16) = acc[i][j];
  __syncthreads();
#pragma unroll
  for (int j = 0; j < 5; ++j) {
    f32x4 s = *(const f32x4*)(red + ((0 * 20 + wave * 5 + j) * 64 + lane) * 16);
#pragma unroll
    for (int w = 1; w < 4; ++w) s += *(const f32x4*)(red + ((w * 20 + wave * 5 + j) * 64 + lane) * 16);
    const int col = nb * 80 + j * 16 + g * 4;
    const u32x2 xv = xres[j];
    u32x2 o;
    o[0] = pack2(bflo(xv[0]) + s[0], bfhi(xv[0]) + s[1]);
    o[1] = pack2(bflo(xv[1]) + s[2], bfhi(xv[1]) + s[3]);
    *(u32x2*)(out + (size_t)row * D_ + col) = o;
  }
  STAMP(3);
}

}

extern "C" void kernel_launch(void* const* d_in, const int* in_sizes, int n_in, void* d_out, int out_size,
                              void* d_ws, size_t ws_size, hipStream_t stream) {
  const u16* x = (const u16*)d_in[0];
  const u16* wqkv = (const u16*)d_in[1];
  const u16* wo = (const u16*)d_in[2];
  const u16* w1 = (const u16*)d_in[4];
  const u16* w3 = (const u16*)d_in[5];
  const u16* w2 = (const u16*)d_in[6];
  k1_kernel<<<256, 768, 0, stream>>>(x, wqkv, wo, w1, w3, w2, (char*)d_ws);
  k2_kernel<<<256, 256, 0, stream>>>(x, (const char*)d_ws, (u16*)d_out);
}
```
